```python
import torch

def _softmax(x, axis=-1):
    return torch.softmax(x, dim=axis)

def _gelu(x):
    return torch.nn.functional.gelu(x, approximate='tanh')

def _silu(x):
    return torch.nn.functional.silu(x)

def _sigmoid(x):
    return torch.sigmoid(x)

def _layernorm(x, axis=-1, eps=1e-05):
    m = x.mean(dim=axis, keepdim=True)
    v = x.var(dim=axis, keepdim=True, unbiased=False)
    return (x - m) / torch.sqrt(v + eps)

def _scatter_add(tgt, idx, src, axis=0):
    return tgt.clone().index_add_(axis, idx, src)

def _segment_sum(src, seg, n):
    out = torch.zeros((n,) + src.shape[1:], dtype=src.dtype)
    return out.index_add_(0, seg, src)

def _pad(x, pw):
    flat = tuple((v for pair in reversed(pw) for v in pair))
    return torch.nn.functional.pad(x, flat)
kernel_name = 'interleave_layers__sliding_local_dilated__mixer_gmlp__prompt'

def setup_inputs():

    _w = lambda a: (lambda t: t.to(torch.bfloat16) if t.is_floating_point() else t)(torch.as_tensor(a))

    def _A_setup():
        B, S, D = (1, 2048, 512)
        return {'x': _w((lambda *s: torch.randn(*s, dtype=torch.bfloat16) * 0.05)(B, S, D)), 'wqkv': _w((lambda *s: torch.randn(*s, dtype=torch.bfloat16) * 0.1)(D, 3 * D)), 'wo': _w((lambda *s: torch.randn(*s, dtype=torch.bfloat16) * 0.1)(D, D))}
    _A_in = _A_setup()

    def _B_setup():
        B, S, D, F = (1, 2048, 512, 1376)
        Fh = F // 2
        return {'x': _w((lambda *s: torch.randn(*s, dtype=torch.bfloat16) * 0.05)(B, S, D)), 'w_in': _w((lambda *s: torch.randn(*s, dtype=torch.bfloat16) * 0.4)(D, 2 * Fh)), 'w_sp': _w(0.01 * (lambda *s: torch.randn(*s, dtype=torch.bfloat16) * 0.4)(S, S)), 'b_sp': _w(1.0 + 0.01 * (lambda *s: torch.randn(*s, dtype=torch.bfloat16) * 0.05)(S)), 'w_out': _w((lambda *s: torch.randn(*s, dtype=torch.bfloat16) * 0.05)(Fh, D))}
    _B_in = _B_setup()
    return {'A_x': _A_in['x'], 'A_wqkv': _A_in['wqkv'], 'A_wo': _A_in['wo'], 'B_x': _B_in['x'], 'B_w_in': _B_in['w_in'], 'B_w_sp': _B_in['w_sp'], 'B_b_sp': _B_in['b_sp'], 'B_w_out': _B_in['w_out']}

def reference(A_x, A_wqkv, A_wo, B_x, B_w_in, B_w_sp, B_b_sp, B_w_out):

    def _A_ref(x, wqkv, wo):
        B, S, D = x.shape
        H = 32
        Dh = D // H
        W, dil = (8, 2)
        qkv = (lambda x, *d: x.permute(*d))((x @ wqkv).reshape(B, S, 3, H, Dh), 2, 0, 3, 1, 4)
        q, k, v = (qkv[0], qkv[1], qkv[2])
        scale = Dh ** (-0.5)
        pos = torch.arange(S)
        off = torch.arange(W) * dil
        idx = pos[:, None] - off[None, :]
        mask = idx >= 0
        idx_c = torch.where(mask, idx, 0).reshape(-1)
        kg = k[:, :, idx_c, :].reshape(B, H, S, W, Dh)
        vg = v[:, :, idx_c, :].reshape(B, H, S, W, Dh)
        logits = torch.einsum('bhsd,bhswd->bhsw', q, kg) * scale
        logits = torch.where(mask[None, None, :, :], logits, -1000000000.0)
        attn = _softmax(logits, axis=-1)
        o = torch.einsum('bhsw,bhswd->bhsd', attn, vg)
        return (lambda x, *d: x.permute(*d))(o, 0, 2, 1, 3).reshape(B, S, D) @ wo

    def _B_ref(x, w_in, w_sp, b_sp, w_out):
        B, S, D = x.shape
        F = w_in.shape[-1]
        Fh = F // 2
        z = _gelu(torch.einsum('bsd,df->bsf', x, w_in))
        z1 = z[..., :Fh].clone()
        z2 = _layernorm(z[..., Fh:], axis=-1)
        g = torch.einsum('ts,bsf->btf', w_sp, z2) + b_sp[None, :, None]
        return torch.einsum('bsf,fd->bsd', z1 * g, w_out)
    x = A_x
    x = x + _A_ref(_layernorm(x, axis=-1), A_wqkv, A_wo)
    x = x + _B_ref(_layernorm(x, axis=-1), B_w_in, B_w_sp, B_b_sp, B_w_out)
    x = x + _A_ref(_layernorm(x, axis=-1), A_wqkv, A_wo)
    x = x + _B_ref(_layernorm(x, axis=-1), B_w_in, B_w_sp, B_b_sp, B_w_out)
    return x
```

```cpp
#include <hip/hip_runtime.h>
#include <hip/hip_bf16.h>
#include <stdint.h>

namespace naive {
typedef unsigned short u16;
__device__ __forceinline__ float bf2f(u16 v){ return __uint_as_float(((unsigned)v)<<16); }
__device__ __forceinline__ u16 f2bf(float f){ unsigned u=__float_as_uint(f); u += 0x7FFF + ((u>>16)&1); return (u16)(u>>16); }
__device__ __forceinline__ float rbf(float f){ return bf2f(f2bf(f)); }

__global__ void ln_k(const u16* in, int ld, int c0, int n, u16* out){
  int r=blockIdx.x; __shared__ float red[256];
  float s=0; for(int i=threadIdx.x;i<n;i+=256) s+=bf2f(in[(size_t)r*ld+c0+i]);
  red[threadIdx.x]=s; __syncthreads();
  for(int o=128;o>0;o>>=1){ if(threadIdx.x<o) red[threadIdx.x]+=red[threadIdx.x+o]; __syncthreads(); }
  float mean=red[0]/n; __syncthreads();
  float q=0; for(int i=threadIdx.x;i<n;i+=256){ float d=bf2f(in[(size_t)r*ld+c0+i])-mean; q+=d*d; }
  red[threadIdx.x]=q; __syncthreads();
  for(int o=128;o>0;o>>=1){ if(threadIdx.x<o) red[threadIdx.x]+=red[threadIdx.x+o]; __syncthreads(); }
  float var=red[0]/n;
  float mb=rbf(mean), vb=rbf(var);
  float sd=rbf(sqrtf(rbf(vb+1e-5f)));
  for(int i=threadIdx.x;i<n;i+=256){ float d=rbf(bf2f(in[(size_t)r*ld+c0+i])-mb); out[(size_t)r*n+i]=f2bf(d/sd); }
}
__global__ void gemm_k(const u16* A,int lda,const u16* B,int ldb,u16* C,int M,int N,int K,int mode,const u16* res){
  int n=blockIdx.x*64+ (threadIdx.x&63); int m=blockIdx.y*4+(threadIdx.x>>6);
  if(n>=N||m>=M) return;
  float acc=0; for(int k=0;k<K;++k) acc+=bf2f(A[(size_t)m*lda+k])*bf2f(B[(size_t)k*ldb+n]);
  float v=rbf(acc);
  if(mode==1){ float x=v; float t=tanhf(0.7978845608028654f*(x+0.044715f*x*x*x)); v=0.5f*x*(1.f+t); }
  if(mode==2){ v=bf2f(res[(size_t)m*N+n])+v; }
  C[(size_t)m*N+n]=f2bf(v);
}
__global__ void attn_k(const u16* qkv,u16* o){
  int idx=blockIdx.x*256+threadIdx.x; int s=idx>>5,h=idx&31; if(s>=2048) return;
  float q[16]; for(int d=0;d<16;++d) q[d]=bf2f(qkv[(size_t)s*1536+h*16+d]);
  float lg[8]; float mx=-3e38f;
  for(int w=0;w<8;++w){ int j=s-2*w; float l; if(j>=0){ float a=0; for(int d=0;d<16;++d) a+=q[d]*bf2f(qkv[(size_t)j*1536+512+h*16+d]); l=rbf(rbf(a)*0.25f);} else l=rbf(-1e9f); lg[w]=l; mx=fmaxf(mx,l);}
  float sum=0; for(int w=0;w<8;++w){ lg[w]=expf(lg[w]-mx); sum+=lg[w]; }
  float acc[16]; for(int d=0;d<16;++d) acc[d]=0;
  for(int w=0;w<8;++w){ int j=s-2*w; if(j<0) j=0; float p=rbf(lg[w]/sum); for(int d=0;d<16;++d) acc[d]+=p*bf2f(qkv[(size_t)j*1536+1024+h*16+d]); }
  for(int d=0;d<16;++d) o[(size_t)s*512+h*16+d]=f2bf(acc[d]);
}
__global__ void gate_k(const u16* z,const u16* g,const u16* b,u16* u){
  int idx=blockIdx.x*256+threadIdx.x; if(idx>=2048*688) return; int t=idx/688,f=idx%688;
  float gg=rbf(bf2f(g[idx])+bf2f(b[t])); u[idx]=f2bf(bf2f(z[(size_t)t*1376+f])*gg);
}

struct Dbg { u16 *ln1,*qkv,*o,*x1,*ln2,*z,*z2n,*g,*u,*x2; };

static void run(void* const* d_in, void* d_out, void* d_ws, hipStream_t stream, Dbg* dbg=nullptr){
  const u16* x=(const u16*)d_in[0]; const u16* wqkv=(const u16*)d_in[1]; const u16* wo=(const u16*)d_in[2];
  const u16* w_in=(const u16*)d_in[4]; const u16* w_sp=(const u16*)d_in[5]; const u16* b_sp=(const u16*)d_in[6]; const u16* w_out=(const u16*)d_in[7];
  u16* ws=(u16*)d_ws; size_t o_=0; auto al=[&](size_t n){ u16* p=ws+o_; o_+=n; return p; };
  u16* ln=al(2048*512); u16* qkv=al(2048*1536); u16* o=al(2048*512); u16* x1=al(2048*512); u16* ln2=al(2048*512); u16* z=al(2048*1376); u16* z2n=al(2048*688); u16* g=al(2048*688); u16* u=al(2048*688); u16* x2=al(2048*512);
  if(dbg){ dbg->ln1=ln; dbg->qkv=qkv; dbg->o=o; dbg->x1=x1; dbg->ln2=ln2; dbg->z=z; dbg->z2n=z2n; dbg->g=g; dbg->u=u; dbg->x2=x2; }
  const u16* xin=x;
  for(int L=0;L<2;++L){
    u16* xo = L==0? x2 : (u16*)d_out;
    ln_k<<<2048,256,0,stream>>>(xin,512,0,512,ln);
    gemm_k<<<dim3(24,512),256,0,stream>>>(ln,512,wqkv,1536,qkv,2048,1536,512,0,nullptr);
    attn_k<<<256,256,0,stream>>>(qkv,o);
    gemm_k<<<dim3(8,512),256,0,stream>>>(o,512,wo,512,x1,2048,512,512,2,xin);
    ln_k<<<2048,256,0,stream>>>(x1,512,0,512,ln2);
    gemm_k<<<dim3(22,512),256,0,stream>>>(ln2,512,w_in,1376,z,2048,1376,512,1,nullptr);
    ln_k<<<2048,256,0,stream>>>(z,1376,688,688,z2n);
    gemm_k<<<dim3(11,512),256,0,stream>>>(w_sp,2048,z2n,688,g,2048,688,2048,0,nullptr);
    gate_k<<<(2048*688+255)/256,256,0,stream>>>(z,g,b_sp,u);
    gemm_k<<<dim3(8,512),256,0,stream>>>(u,688,w_out,512,xo,2048,512,688,2,x1);
    xin=xo;
  }
}
}

extern "C" void kernel_launch(void* const* d_in, const int* in_sizes, int n_in, void* d_out, int out_size, void* d_ws, size_t ws_size, hipStream_t stream){
  naive::run(d_in,d_out,d_ws,stream);
}
```

```cpp
#include <hip/hip_runtime.h>
#include <hip/hip_bf16.h>
#include <stdint.h>
#include <type_traits>

#define NLAYERS 2
typedef unsigned short u16;
typedef unsigned long long u64;
typedef __attribute__((ext_vector_type(8))) short bf16x8;
typedef __attribute__((ext_vector_type(4))) short s16x4;
typedef __attribute__((ext_vector_type(4))) float f32x4;
typedef __attribute__((ext_vector_type(2))) __bf16 bf16x2;
typedef __attribute__((ext_vector_type(4))) unsigned u32x4;
typedef __attribute__((ext_vector_type(2))) unsigned u32x2;
typedef __attribute__((address_space(3))) const char* lds_cptr;
typedef short v4i16_t __attribute__((ext_vector_type(4)));
#define DEV __device__ __forceinline__

constexpr int SEQ = 2048, DM = 512, FH = 688, FF = 1376, UP = 704;

template <int I, int N, class F> DEV void static_for(F&& f) {
  if constexpr (I < N) { f(std::integral_constant<int, I>{}); static_for<I + 1, N>(f); }
}
DEV void glds16(const void* g, unsigned lds_base) {
  unsigned sv;
  asm volatile("s_mov_b32 %0, m0\n\ts_mov_b32 m0, %2\n\ts_nop 0\n\tglobal_load_lds_dwordx4 %1, off\n\ts_mov_b32 m0, %0"
               : "=&s"(sv) : "v"(g), "s"(lds_base) : "memory");
}
template <int IMM> DEV void glds16s(unsigned voff, const void* sbase, unsigned ldsbase) {
  asm volatile("s_add_u32 m0, %1, %2\n\ts_nop 0\n\tglobal_load_lds_dwordx4 %0, %3" ::"v"(voff), "s"(ldsbase), "i"(IMM), "s"(sbase) : "memory", "scc");
}
template <int N> DEV void wait_bar() { asm volatile("s_waitcnt vmcnt(%0) lgkmcnt(0)\n\ts_barrier" ::"i"(N) : "memory"); }
DEV s16x4 trread(lds_cptr p) {
  return __builtin_bit_cast(s16x4, __builtin_amdgcn_ds_read_tr16_b64_v4i16((__attribute__((address_space(3))) v4i16_t*)p));
}
DEV float bf2f(unsigned v) { return __uint_as_float(v << 16); }
DEV unsigned cvtpk(float lo, float hi) { bf16x2 v = {(__bf16)lo, (__bf16)hi}; return __builtin_bit_cast(unsigned, v); }
DEV float rbf(float f) { return __uint_as_float(cvtpk(f, f) << 16); }
DEV float dot2(unsigned a, unsigned b, float c) {
  return __builtin_amdgcn_fdot2_f32_bf16(__builtin_bit_cast(bf16x2, a), __builtin_bit_cast(bf16x2, b), c, false);
}
template <int CTRL> DEV float dpp(float v) { return __int_as_float(__builtin_amdgcn_mov_dpp(__float_as_int(v), CTRL, 0xF, 0xF, true)); }
DEV float sum_x1(float v) { return v + dpp<0xB1>(v); }
DEV float sum_x2(float v) { return v + dpp<0x4E>(v); }
DEV float sum_x4(float v) { return v + dpp<0x141>(v); }
DEV float sum_g(float v) {
  auto a = __builtin_amdgcn_permlane16_swap(__float_as_uint(v), __float_as_uint(v), false, false);
  v = __uint_as_float(a[0]) + __uint_as_float(a[1]);
  auto b = __builtin_amdgcn_permlane32_swap(__float_as_uint(v), __float_as_uint(v), false, false);
  return __uint_as_float(b[0]) + __uint_as_float(b[1]);
}
DEV float max_g(float v) {
  auto a = __builtin_amdgcn_permlane16_swap(__float_as_uint(v), __float_as_uint(v), false, false);
  v = fmaxf(__uint_as_float(a[0]), __uint_as_float(a[1]));
  auto b = __builtin_amdgcn_permlane32_swap(__float_as_uint(v), __float_as_uint(v), false, false);
  return fmaxf(__uint_as_float(b[0]), __uint_as_float(b[1]));
}
#define SB0() __builtin_amdgcn_sched_barrier(0)
#define MFMA16(a, b, c) __builtin_amdgcn_mfma_f32_16x16x32_bf16(a, b, c, 0, 0, 0)

#define STAMP(KID, IDX) do { } while (0)
DEV void l2_touch(const void* p, int bytes, int lane, unsigned junk_lds) {
  return;
  const char* a = (const char*)p + lane * 128;
#pragma unroll 1
  for (int off = lane * 128; off < bytes; off += 8192, a += 8192) {
    unsigned sv;
    asm volatile("s_mov_b32 %0, m0\n\ts_mov_b32 m0, %2\n\ts_nop 0\n\tglobal_load_lds_dword %1, off\n\ts_mov_b32 m0, %0"
                 : "=&s"(sv) : "v"(a), "s"(junk_lds) : "memory");
  }
}
template <int WRB> DEV int wswz(int k) {
  if constexpr (WRB % 256 == 128) return ((k >> 1) & 1) | (((k >> 3) & 1) << 1);
  else return (k >> 3) & 1;
}

template <int N> DEV void wait_vm() { asm volatile("s_waitcnt vmcnt(%0)" ::"i"(N) : "memory"); }
DEV void bar() { asm volatile("s_waitcnt lgkmcnt(0)\n\ts_barrier" ::: "memory"); }

template <int XR, int WRB, int NK, int S, int R, int MBW, int NBW, int LN, int KCLAMP, bool VT = false>
struct Core {
  static constexpr int XI = (XR * 2 + 63) / 64, WI = WRB / 64, LPS = XI + WI;
  static constexpr int XB = XR * 128, STG = XB + 64 * WRB, RING = S * STG, NR = (NK + R - 1) / R;
  static constexpr int JUNK = RING + XR * 8, FLG = JUNK + 256, LDS_BYTES = FLG + 32;
  static constexpr int D = 2;
  unsigned xo[XI], wo_[WI]; int wk[WI];
  const char* xb; const char* wb;
  unsigned ldsx, ldsw; int ldw; int lane;

  template <class FR, class FC>
  DEV void init(unsigned lds0, int wid, int lane_, const u16* X, int ldx, const u16* W, int ldw_, FR xrow, FC wcol) {
    lane = lane_; ldw = ldw_; xb = (const char*)X; wb = (const char*)W;
    ldsx = lds0 + wid * (XR * 32); ldsw = lds0 + XB + wid * (16 * WRB);
#pragma unroll
    for (int i = 0; i < XI; ++i) {
      int ci = i * 64 + lane; if (ci >= XR * 2) ci = XR * 2 - 1;
      int r = wid * (XR / 4) + (ci >> 3), pos = ci & 7;
      xo[i] = (unsigned)(xrow(r) * ldx + ((pos ^ (r & 7)) << 3)) * 2u;
    }
#pragma unroll
    for (int i = 0; i < WI; ++i) {
      int b = i * 1024 + lane * 16; int kr = wid * 16 + b / WRB, ir = b % WRB;
      int u = (ir >> 5) ^ wswz<WRB>(kr);
      wk[i] = kr;
      wo_[i] = (unsigned)(kr * ldw + wcol(u) + ((ir >> 4) & 1) * 8) * 2u;
    }
  }
  template <class P> DEV void issue(int st, unsigned sb, P&& poll) {
    const unsigned lx = ldsx + sb, lw = ldsw + sb;
    static_for<0, XI>([&](auto ic) {
      constexpr int i = ic.value;
      if constexpr ((XR * 2) % 64 != 0 && i == XI - 1) { if (lane < (XR * 2) % 64) glds16s<i * 1024>(xo[i], xb, lx); }
      else glds16s<i * 1024>(xo[i], xb, lx);
    });
    poll(st * LPS + XI);
    if constexpr (KCLAMP > 0) {
      if (st == NK - 1) {
#pragma unroll
        for (int i = 0; i < WI; ++i) { int over = st * 64 + wk[i] - (KCLAMP - 1); if (over > 0) wo_[i] -= (unsigned)(over * ldw) * 2u; }
      }
    }
    static_for<0, WI>([&](auto ic) {
      constexpr int i = ic.value;
      glds16s<i * 1024>(wo_[i], wb, lw);
      if constexpr (i % 3 == 2 || i == WI - 1) poll(st * LPS + XI + i + 1);
    });
    xb += 128; wb += (size_t)128 * ldw;
  }
  static DEV void wait_stages(int n) {
    static_for<0, S>([&](auto rc) { if (n == rc.value) wait_vm<rc.value * LPS>(); });
  }
  static DEV unsigned min4(const char* p) {
    u32x4 v; asm volatile("ds_read_b128 %0, %1\n\ts_waitcnt lgkmcnt(0)" : "=&v"(v) : "v"((unsigned)(uintptr_t)p) : "memory");
    unsigned m = min(min(v[0], v[1]), min(v[2], v[3]));
    return __builtin_amdgcn_readfirstlane(m);
  }
  static DEV void flag_store(unsigned addr, unsigned v) { asm volatile("ds_write_b32 %0, %1" :: "v"(addr), "v"(v) : "memory"); }
  static DEV void flags_init(char* lds) { if (threadIdx.x < 8) ((unsigned*)(lds + FLG))[threadIdx.x] = 0u; bar(); }
  DEV void load_loop(char* lds, int wid, int kid) {
    static_assert(S <= NK && D < S, "ring shape");
    const unsigned landed = (unsigned)(uintptr_t)(lds + FLG) + wid * 4;
    const char* done = lds + FLG + 16;
    unsigned sb = 0; int pub = 0;
    f32x4 s1[XI] = {}, s2[XI] = {};
    auto publish = [&](int n) {
      if constexpr (LN == 2) {
#pragma unroll 1
        for (int st = pub; st < n; ++st) {
          const unsigned cb = (unsigned)(st % S) * STG;
#pragma unroll
          for (int i = 0; i < XI; ++i) {
            u32x4 v = *(const u32x4*)(lds + cb + wid * (XR * 32) + i * 1024 + lane * 16);
#pragma unroll
            for (int j = 0; j < 4; ++j) {
              float lo = __uint_as_float(v[j] << 16), hi = __uint_as_float(v[j] & 0xFFFF0000u);
              s1[i][j] += lo + hi; s2[i][j] = fmaf(lo, lo, fmaf(hi, hi, s2[i][j]));
            }
          }
        }
      }
      if (n > pub) { pub = n; flag_store(landed, (unsigned)n); }
    };
    auto poll = [&](int ops) {
      unsigned x; asm volatile("s_getreg_b32 %0, hwreg(HW_REG_IB_STS)" : "=s"(x) :: "memory");
      const int vm = (int)((x & 15u) | ((x >> 18) & 48u));
      publish((ops - vm) / LPS);
    };
    unsigned dn = 0;
#pragma unroll 1
    for (int st = 0; st < NK; ++st) {
      if (st >= S) {
        const unsigned need = st - S + 1;
        while (dn < need) { dn = min4(done); poll(st * LPS); }
      }
      issue(st, sb, poll); sb += STG; if (sb == RING) sb = 0;
    }
    while (pub < NK) poll(NK * LPS);
    if constexpr (LN == 2) {
      float* st = (float*)(lds + RING);
#pragma unroll
      for (int i = 0; i < XI; ++i) {
        float a = (s1[i][0] + s1[i][1]) + (s1[i][2] + s1[i][3]), b = (s2[i][0] + s2[i][1]) + (s2[i][2] + s2[i][3]);
        a = sum_x4(sum_x2(sum_x1(a))); b = sum_x4(sum_x2(sum_x1(b)));
        float mean = a * (1.f / DM), var = fmaxf(b * (1.f / DM) - mean * mean, 0.f);
        int ci = i * 64 + lane;
        if ((lane & 7) == 0 && ci < XR * 2) { int r = wid * (XR / 4) + (ci >> 3); st[r * 2] = mean; st[r * 2 + 1] = rsqrtf(var + 1e-5f); }
      }
    }
    if constexpr (LN != 0) bar();
  }
  template <class FR> static DEV void stats_from(char* lds, int tid, const float* part, FR xrow) {
    float* st = (float*)(lds + RING);
#pragma unroll
    for (int r = tid >> 2; r < XR; r += 64) {
      const f32x4* p = (const f32x4*)(part + (size_t)xrow(r) * 32 + (tid & 3) * 8);
      f32x4 a = p[0], b = p[1];
      float s = (a[0] + a[2]) + (b[0] + b[2]), q = (a[1] + a[3]) + (b[1] + b[3]);
      s = sum_x2(sum_x1(s)); q = sum_x2(sum_x1(q));
      float mean = s * (1.f / DM), var = fmaxf(q * (1.f / DM) - mean * mean, 0.f);
      if ((tid & 3) == 0) { st[r * 2] = mean; st[r * 2 + 1] = rsqrtf(var + 1e-5f); }
    }
  }
  struct Frags { bf16x8 x[2][MBW], w[2][NBW]; };
  static DEV void run(char* lds, int wid, int lane, int xr0, const int (&wu)[NBW], f32x4 (&acc)[NBW][MBW], f32x4 (&cs)[NBW], int kid) {
    const int g = lane >> 4, q = (lane & 15) >> 2, p = lane & 3;
    lds_cptr L = (lds_cptr)lds;
    const int xa0 = (xr0 + (lane & 15)) * 128 + ((g ^ (lane & 7)) << 4);
    int wa[NBW];
#pragma unroll
    for (int nb = 0; nb < NBW; ++nb) wa[nb] = XB + (8 * g + q) * WRB + ((wu[nb] ^ wswz<WRB>(8 * g + q)) << 5) + p * 8;
    bf16x8 ones;
#pragma unroll
    for (int j = 0; j < 8; ++j) ones[j] = 0x3F80;
    int sb = 0;
    auto ld = [&](Frags& f) {
#pragma unroll
      for (int ks = 0; ks < 2; ++ks) {
#pragma unroll
        for (int nb = 0; nb < NBW; ++nb) {
          s16x4 lo = trread(L + sb + wa[nb] + (32 * ks) * WRB), hi = trread(L + sb + wa[nb] + (32 * ks + 4) * WRB);
          f.w[ks][nb] = __builtin_shufflevector(lo, hi, 0, 1, 2, 3, 4, 5, 6, 7);
        }
#pragma unroll
        for (int mb = 0; mb < MBW; ++mb)
          f.x[ks][mb] = *(const __attribute__((address_space(3))) bf16x8*)(L + sb + ((xa0 ^ (ks * 64)) + mb * 2048));
      }
      sb += STG; if (sb == RING) sb = 0;
    };
    auto mm = [&](const Frags& f) {
#pragma unroll
      for (int ks = 0; ks < 2; ++ks)
#pragma unroll
        for (int nb = 0; nb < NBW; ++nb) {
#pragma unroll
          for (int mb = 0; mb < MBW; ++mb) {
            if (VT && nb == NBW - 1) acc[nb][mb] = MFMA16(f.x[ks][mb], f.w[ks][nb], acc[nb][mb]);
            else acc[nb][mb] = MFMA16(f.w[ks][nb], f.x[ks][mb], acc[nb][mb]);
          }
          if constexpr (LN != 0) {
            if (VT && nb == NBW - 1) cs[nb] = MFMA16(ones, f.w[ks][nb], cs[nb]);
            else cs[nb] = MFMA16(f.w[ks][nb], ones, cs[nb]);
          }
        }
    };
    constexpr int NM = 2 * NBW * (MBW + (LN != 0 ? 1 : 0)), NRD = 2 * (2 * NBW + MBW), RPM = (NRD + NM - 1) / NM;
    auto step = [&](Frags& nx, const Frags& cu) {
      SB0(); ld(nx); mm(cu);
#pragma unroll
      for (int i = 0; i < NM; ++i) { __builtin_amdgcn_sched_group_barrier(0x008, 1, 0); __builtin_amdgcn_sched_group_barrier(0x100, RPM, 0); }
      SB0();
    };
    Frags A, B;
    const char* landed = lds + FLG;
    const unsigned done = (unsigned)(uintptr_t)(lds + FLG + 16) + wid * 4;
    unsigned fl = 0;
    auto sync = [&](int sdone, int need) {
      asm volatile("s_waitcnt lgkmcnt(0)" ::: "memory");
      flag_store(done, (unsigned)sdone);
      if (fl < (unsigned)need) { fl = min4(landed); while (fl < (unsigned)need) { __builtin_amdgcn_s_sleep(1); fl = min4(landed); } }
    };
    sync(0, 1); ld(A);
    if (wid == 0) STAMP(kid, 1);
#pragma unroll 1
    for (int s = 0; s + 2 < NK; s += 2) {
      sync(s + 1, s + 2); step(B, A);
      sync(s + 2, s + 3); step(A, B);
    }
    if constexpr (NK % 2 == 0) { sync(NK - 1, NK); step(B, A); mm(B); }
    else { mm(A); }
    if constexpr (LN != 0) bar();
  }
};

DEV void tile_of(int& rt, int& ct) { int b = blockIdx.x, xcd = b & 7, idx = b >> 3; rt = xcd * 4 + (idx & 3); ct = idx >> 2; }
DEV void warm_share(const void* p, int bytes, int w4, int lane, unsigned junk) { const int sh = bytes / 128; l2_touch((const char*)p + (size_t)((blockIdx.x >> 3) * 4 + w4) * sh, sh, lane, junk); }

DEV s16x4 pack4(f32x4 v) { u32x2 p = {cvtpk(v[0], v[1]), cvtpk(v[2], v[3])}; return __builtin_bit_cast(s16x4, p); }
#define MFMA16K16(a, b, c) __builtin_amdgcn_mfma_f32_16x16x16bf16_1k(a, b, c, 0, 0, 0)
template <int LNM>
__global__ __launch_bounds__(512, 2) void k1_qkv_attn(const u16* __restrict__ x, const float* __restrict__ part, const u16* __restrict__ wqkv, u16* __restrict__ o, u64* zero_buf, const u16* __restrict__ wnext, int kid) {
  if (threadIdx.x == 0) STAMP(kid, 0);
  using C = Core<80, 384, 8, 4, 1, 5, 3, LNM, 0, true>;
  __shared__ __attribute__((aligned(16))) char lds[C::LDS_BYTES];
  const int tid = threadIdx.x, lane = tid & 63, wid = __builtin_amdgcn_readfirstlane(tid >> 6);
  int rt, ct; tile_of(rt, ct); const int r0 = rt * 64;
  C::flags_init(lds);
  if (zero_buf && tid < 128) zero_buf[blockIdx.x * 128 + tid] = 0ull;
  if (wid >= 4) {
    C c; c.init((unsigned)(uintptr_t)lds, wid - 4, lane, x, DM, wqkv, 3 * DM,
              [&](int r) { int gr = r0 - 16 + r; return gr < 0 ? 0 : gr; },
              [&](int u) { return (u >> 2) * DM + (ct * 4 + (u & 3)) * 16; });
    c.load_loop(lds, wid - 4, kid);
    if (wid == 4) STAMP(kid, 2);
    return;
  }
  if constexpr (LNM == 1) C::stats_from(lds, tid, part, [&](int r) { int gr = r0 - 16 + r; return gr < 0 ? 0 : gr; });
  f32x4 acc[3][5] = {}, cs[3] = {};
  const int wu[3] = {wid, 4 + wid, 8 + wid};
  C::run(lds, wid, lane, 0, wu, acc, cs, kid); if (wid == 0) STAMP(kid, 3);
  const float* stt = (const float*)(lds + C::RING);
  const int g = lane >> 4, m16 = lane & 15;
  s16x4 qf[5], kf[5], vf[5];
#pragma unroll
  for (int mb = 0; mb < 5; ++mb) {
    const float rs = stt[(mb * 16 + m16) * 2 + 1], cm = -rs * stt[(mb * 16 + m16) * 2];
    if (mb > 0) qf[mb] = pack4(rs * acc[0][mb] + cm * cs[0]);
    kf[mb] = pack4(rs * acc[1][mb] + cm * cs[1]);
    const f32x4 sa = *(const f32x4*)(stt + (mb * 16 + 4 * g) * 2), sb2 = *(const f32x4*)(stt + (mb * 16 + 4 * g) * 2 + 4);
    f32x4 v = {sa[1] * (acc[2][mb][0] - sa[0] * cs[2][0]), sa[3] * (acc[2][mb][1] - sa[2] * cs[2][0]),
               sb2[1] * (acc[2][mb][2] - sb2[0] * cs[2][0]), sb2[3] * (acc[2][mb][3] - sb2[2] * cs[2][0])};
    vf[mb] = pack4(v);
  }
  bool v0[4], v1[4];
#pragma unroll
  for (int j = 0; j < 4; ++j) { int d0 = m16 + 16 - (4 * g + j), d1 = m16 - (4 * g + j); v0[j] = d0 <= 14 && !(d0 & 1); v1[j] = d1 >= 0 && d1 <= 14 && !(d1 & 1); }
  const float sc = 0.25f * 1.4426950408889634f;
#pragma unroll
  for (int qb = 0; qb < 4; ++qb) {
    f32x4 s0 = MFMA16K16(kf[qb], qf[qb + 1], (f32x4{})), s1 = MFMA16K16(kf[qb + 1], qf[qb + 1], (f32x4{}));
    const bool prev_ok = (r0 + qb * 16) > 0;
    float mx = -3.0e38f;
#pragma unroll
    for (int j = 0; j < 4; ++j) {
      s0[j] = (v0[j] && prev_ok) ? s0[j] * sc : -1.0e9f; s1[j] = v1[j] ? s1[j] * sc : -1.0e9f;
      mx = fmaxf(mx, fmaxf(s0[j], s1[j]));
    }
    mx = max_g(mx);
    float sum = 0.f;
#pragma unroll
    for (int j = 0; j < 4; ++j) { s0[j] = __builtin_amdgcn_exp2f(s0[j] - mx); s1[j] = __builtin_amdgcn_exp2f(s1[j] - mx); sum += s0[j] + s1[j]; }
    sum = sum_g(sum);
    const float inv = __builtin_amdgcn_rcpf(sum);
    f32x4 ot = MFMA16K16(vf[qb], pack4(s0 * inv), (f32x4{}));
    ot = MFMA16K16(vf[qb + 1], pack4(s1 * inv), ot);
    *(u32x2*)(o + (size_t)(r0 + qb * 16 + m16) * DM + (ct * 4 + wid) * 16 + 4 * g) = u32x2{cvtpk(ot[0], ot[1]), cvtpk(ot[2], ot[3])};
  }
  if (wid == 0) STAMP(kid, 4);
}

template <int NK, int LDA, int KCLAMP>
__global__ __launch_bounds__(512, 2) void k_proj_res(const u16* __restrict__ a, const u16* __restrict__ w, const u16* __restrict__ res, u16* __restrict__ out, float* __restrict__ part, const u16* __restrict__ wnext, int wnext_bytes, int kid) {
  if (threadIdx.x == 0) STAMP(kid, 0);
  using C = Core<64, 128, NK, 8, 2, 2, 2, 0, KCLAMP>;
  __shared__ __attribute__((aligned(16))) char lds[C::LDS_BYTES];
  const int tid = threadIdx.x, lane = tid & 63, wid = __builtin_amdgcn_readfirstlane(tid >> 6);
  int rt, ct; tile_of(rt, ct); const int r0 = rt * 64, c0 = ct * 64;
  C::flags_init(lds);
  if (wid >= 4) {
    C c; c.init((unsigned)(uintptr_t)lds, wid - 4, lane, a, LDA, w, DM, [&](int r) { return r0 + r; }, [&](int u) { return c0 + u * 16; });
    c.load_loop(lds, wid - 4, kid);
    if (wnext) warm_share(wnext, wnext_bytes, wid - 4, lane, (unsigned)(uintptr_t)lds + C::JUNK); if (wid == 4) STAMP(kid, 2); return;
  }
  const int wm = wid >> 1, wn = wid & 1, g = lane >> 4, m16 = lane & 15;
  u32x2 rv[2][2];
#pragma unroll
  for (int nb = 0; nb < 2; ++nb)
#pragma unroll
    for (int mb = 0; mb < 2; ++mb) rv[nb][mb] = *(const u32x2*)(res + (size_t)(r0 + wm * 32 + mb * 16 + m16) * DM + c0 + (wn * 2 + nb) * 16 + g * 4);
  f32x4 acc[2][2] = {}, cs[2];
  const int wu[2] = {wn * 2, wn * 2 + 1};
  C::run(lds, wid, lane, wm * 32, wu, acc, cs, kid); if (wid == 0) STAMP(kid, 3);
#pragma unroll
  for (int mb = 0; mb < 2; ++mb) {
    float ps = 0.f, pq = 0.f;
#pragma unroll
    for (int nb = 0; nb < 2; ++nb) {
      f32x4 A = acc[nb][mb]; u32x2 r = rv[nb][mb];
      float v0 = bf2f(r[0] & 0xFFFF) + rbf(A[0]), v1 = bf2f(r[0] >> 16) + rbf(A[1]), v2 = bf2f(r[1] & 0xFFFF) + rbf(A[2]), v3 = bf2f(r[1] >> 16) + rbf(A[3]);
      u32x2 o = {cvtpk(v0, v1), cvtpk(v2, v3)};
      *(u32x2*)(out + (size_t)(r0 + wm * 32 + mb * 16 + m16) * DM + c0 + (wn * 2 + nb) * 16 + g * 4) = o;
      v0 = bf2f(o[0] & 0xFFFF); v1 = bf2f(o[0] >> 16); v2 = bf2f(o[1] & 0xFFFF); v3 = bf2f(o[1] >> 16);
      ps += (v0 + v1) + (v2 + v3); pq += (v0 * v0 + v1 * v1) + (v2 * v2 + v3 * v3);
    }
    ps = sum_g(ps); pq = sum_g(pq);
    if (g == 0) *(float2*)(part + (size_t)(r0 + wm * 32 + mb * 16 + m16) * 32 + (ct * 2 + wn) * 2) = make_float2(ps, pq);
  }
  if (wid == 0) STAMP(kid, 4);
}

typedef __attribute__((ext_vector_type(2))) float f32x2;
DEV f32x2 gelu2(f32x2 t) {
  const float c1 = -1.5957691216057308f * 1.4426950408889634f, c2 = c1 * 0.044715f;
  f32x2 y = t * (t * t * c2 + c1);
  f32x2 d = {__builtin_amdgcn_exp2f(y[0]), __builtin_amdgcn_exp2f(y[1])};
  d += 1.f;
  f32x2 r = {__builtin_amdgcn_rcpf(d[0]), __builtin_amdgcn_rcpf(d[1])};
  return t * r;
}
__global__ __launch_bounds__(512, 2) void k3_win(const u16* __restrict__ x, const float* __restrict__ part, const u16* __restrict__ w_in, u16* __restrict__ z1, u16* __restrict__ z2n, u64* xch, unsigned tag, const u16* __restrict__ w_sp, int kid) {
  if (threadIdx.x == 0) STAMP(kid, 0);
  using C = Core<64, 384, 8, 4, 1, 4, 3, 1, 0>;
  __shared__ __attribute__((aligned(16))) char lds[C::LDS_BYTES];
  const int tid = threadIdx.x, lane = tid & 63, wid = __builtin_amdgcn_readfirstlane(tid >> 6), w4 = wid & 3;
  int rt, ct; tile_of(rt, ct); const int r0 = rt * 64;
  C::flags_init(lds);
  const int half = ct >> 2, zt = ct & 3, nun = zt == 3 ? 10 : 11, f0 = zt * 176;
  f32x4 a2[3][2], cs[3];
  f32x4* ho = (f32x4*)(lds + 4096) + w4 * (9 * 64) + lane;
  if (wid >= 4) {
    C c; c.init((unsigned)(uintptr_t)lds, wid - 4, lane, x, DM, w_in, FF, [&](int r) { return r0 + r; },
              [&](int u) { return half * FH + f0 + (u < nun ? u : 0) * 16; });
    c.load_loop(lds, wid - 4, kid);
    if (wid == 4) STAMP(kid, 2);
    bar();
#pragma unroll
    for (int nb = 0; nb < 3; ++nb) { a2[nb][0] = ho[(nb * 3) * 64]; a2[nb][1] = ho[(nb * 3 + 1) * 64]; cs[nb] = ho[(nb * 3 + 2) * 64]; }
  } else {
    C::stats_from(lds, tid, part, [&](int r) { return r0 + r; });
    f32x4 acc[3][4] = {};
#pragma unroll
    for (int nb = 0; nb < 3; ++nb) cs[nb] = f32x4{};
    const int wu[3] = {wid * 3, wid * 3 + 1, wid * 3 + 2};
    C::run(lds, wid, lane, 0, wu, acc, cs, kid); if (wid == 0) STAMP(kid, 3);
#pragma unroll
    for (int nb = 0; nb < 3; ++nb) { ho[(nb * 3) * 64] = acc[nb][2]; ho[(nb * 3 + 1) * 64] = acc[nb][3]; ho[(nb * 3 + 2) * 64] = cs[nb]; a2[nb][0] = acc[nb][0]; a2[nb][1] = acc[nb][1]; }
    bar();
  }
  const float* stt = (const float*)(lds + C::RING);
  const int g = lane >> 4, m16 = lane & 15, rb = (wid >> 2) * 32;
  u32x2 pk[3][2]; float p1[2], p2[2];
#pragma unroll
  for (int mb = 0; mb < 2; ++mb) {
    const float rs = stt[(rb + mb * 16 + m16) * 2 + 1], cm = -rs * stt[(rb + mb * 16 + m16) * 2];
    f32x2 s1 = {0.f, 0.f}, s2 = {0.f, 0.f};
#pragma unroll
    for (int nb = 0; nb < 3; ++nb) {
      f32x4 t = rs * a2[nb][mb] + cm * cs[nb];
      f32x2 lo = gelu2(f32x2{t[0], t[1]}), hi = gelu2(f32x2{t[2], t[3]});
      pk[nb][mb] = u32x2{cvtpk(lo[0], lo[1]), cvtpk(hi[0], hi[1])};
      if (w4 * 3 + nb < nun) {
        f32x2 a = {bf2f(pk[nb][mb][0] & 0xFFFF), bf2f(pk[nb][mb][0] >> 16)}, b = {bf2f(pk[nb][mb][1] & 0xFFFF), bf2f(pk[nb][mb][1] >> 16)};
        s1 += a + b; s2 += a * a + b * b;
      }
    }
    p1[mb] = s1[0] + s1[1]; p2[mb] = s2[0] + s2[1];
  }
  if (half == 0) {
#pragma unroll
    for (int mb = 0; mb < 2; ++mb)
#pragma unroll
      for (int nb = 0; nb < 3; ++nb)
        if (w4 * 3 + nb < nun) *(u32x2*)(z1 + (size_t)(r0 + rb + mb * 16 + m16) * FH + f0 + (w4 * 3 + nb) * 16 + g * 4) = pk[nb][mb];
    if (wid == 0) STAMP(kid, 4);
    return;
  }
  if (wid == 0) STAMP(kid, 5);
  float* red = (float*)lds;
  float* tot = red + 512;
#pragma unroll
  for (int mb = 0; mb < 2; ++mb) {
    float a = p1[mb], b = p2[mb];
    a = sum_g(a); b = sum_g(b);
    if (g == 0) *(float2*)(red + (w4 * 64 + rb + mb * 16 + m16) * 2) = make_float2(a, b);
  }
  __syncthreads();
  if (tid < 128) {
    float own = (red[tid] + red[128 + tid]) + (red[256 + tid] + red[384 + tid]);
    u64* base = xch + (size_t)rt * 4 * 128;
    __hip_atomic_store(base + zt * 128 + tid, ((u64)tag << 32) | __float_as_uint(own), __ATOMIC_RELAXED, __HIP_MEMORY_SCOPE_AGENT);
    u64 v[4]; int spins = 0; bool ok;
    do {
#pragma unroll
      for (int t = 0; t < 4; ++t) v[t] = __hip_atomic_load(base + t * 128 + tid, __ATOMIC_RELAXED, __HIP_MEMORY_SCOPE_AGENT);
      ok = true;
#pragma unroll
      for (int t = 0; t < 4; ++t) ok = ok && (t == zt || (unsigned)(v[t] >> 32) == tag);
    } while (!ok && ++spins < (1 << 22));
    float pt[4];
#pragma unroll
    for (int t = 0; t < 4; ++t) pt[t] = t == zt ? own : __uint_as_float((unsigned)v[t]);
    tot[tid] = (pt[0] + pt[1]) + (pt[2] + pt[3]);
  }
  __syncthreads();
  if (wid == 0) STAMP(kid, 6);
#pragma unroll
  for (int mb = 0; mb < 2; ++mb) {
    const int r = rb + mb * 16 + m16;
    const float mean = tot[r * 2] * (1.f / FH), var = fmaxf(tot[r * 2 + 1] * (1.f / FH) - mean * mean, 0.f);
    const float rs = rsqrtf(var + 1e-5f), cm = -mean * rs;
#pragma unroll
    for (int nb = 0; nb < 3; ++nb) {
      const int f = f0 + (w4 * 3 + nb) * 16;
      if (w4 * 3 + nb < nun) {
        u32x2 v = pk[nb][mb];
        *(u32x2*)(z2n + ((size_t)(f / 96) * SEQ + r0 + r) * 96 + f % 96 + g * 4) =
            u32x2{cvtpk(fmaf(bf2f(v[0] & 0xFFFF), rs, cm), fmaf(bf2f(v[0] >> 16), rs, cm)), cvtpk(fmaf(bf2f(v[1] & 0xFFFF), rs, cm), fmaf(bf2f(v[1] >> 16), rs, cm))};
      }
    }
  }
  if (wid == 0) STAMP(kid, 4);
}

__global__ __launch_bounds__(512, 2) void k4_wsp(const u16* __restrict__ w_sp, const u16* __restrict__ z2n, const u16* __restrict__ z1, const u16* __restrict__ b_sp, u16* __restrict__ u, const u16* __restrict__ wnext, int kid) {
  if (threadIdx.x == 0) STAMP(kid, 0);
  using C = Core<64, 192, 32, 7, 2, 2, 3, 0, 0>;
  __shared__ __attribute__((aligned(16))) char lds[C::LDS_BYTES];
  const int tid = threadIdx.x, lane = tid & 63, wid = __builtin_amdgcn_readfirstlane(tid >> 6);
  int rt, ct; tile_of(rt, ct); const int r0 = rt * 64, c0 = ct * 96;
  C::flags_init(lds);
  if (wid >= 4) {
    C c; c.init((unsigned)(uintptr_t)lds, wid - 4, lane, w_sp, SEQ, z2n + (size_t)ct * SEQ * 96, 96, [&](int r) { return r0 + r; },
              [&](int uu) { return uu * 16; });
    c.load_loop(lds, wid - 4, kid);
    warm_share(wnext, FH * DM * 2, wid - 4, lane, (unsigned)(uintptr_t)lds + C::JUNK); if (wid == 4) STAMP(kid, 2); return;
  }
  const int wm = wid >> 1, wn = wid & 1, g = lane >> 4, m16 = lane & 15;
  u32x2 zv[3][2]; float bv[2];
#pragma unroll
  for (int mb = 0; mb < 2; ++mb) {
    bv[mb] = bf2f(b_sp[r0 + wm * 32 + mb * 16 + m16]);
#pragma unroll
    for (int nb = 0; nb < 3; ++nb) {
      int col = c0 + (wn * 3 + nb) * 16; if (col >= FH) col = 0;
      zv[nb][mb] = *(const u32x2*)(z1 + (size_t)(r0 + wm * 32 + mb * 16 + m16) * FH + col + g * 4);
    }
  }
  f32x4 acc[3][2] = {}, cs[3];
  const int wu[3] = {wn * 3, wn * 3 + 1, wn * 3 + 2};
  C::run(lds, wid, lane, wm * 32, wu, acc, cs, kid); if (wid == 0) STAMP(kid, 3);
#pragma unroll
  for (int nb = 0; nb < 3; ++nb) {
    const int col = c0 + (wn * 3 + nb) * 16;
#pragma unroll
    for (int mb = 0; mb < 2; ++mb) {
      f32x4 A = acc[nb][mb]; u32x2 z = zv[nb][mb]; float b = bv[mb];
      float v0 = bf2f(z[0] & 0xFFFF) * rbf(rbf(A[0]) + b), v1 = bf2f(z[0] >> 16) * rbf(rbf(A[1]) + b);
      float v2 = bf2f(z[1] & 0xFFFF) * rbf(rbf(A[2]) + b), v3 = bf2f(z[1] >> 16) * rbf(rbf(A[3]) + b);
      u32x2 r = {cvtpk(v0, v1), cvtpk(v2, v3)};
      if (col >= FH) r = u32x2{0u, 0u};
      if (col < UP) *(u32x2*)(u + (size_t)(r0 + wm * 32 + mb * 16 + m16) * UP + col + g * 4) = r;
    }
  }
  if (wid == 0) STAMP(kid, 4);
}

extern "C" void kernel_launch(void* const* d_in, const int* in_sizes, int n_in, void* d_out, int out_size, void* d_ws, size_t ws_size, hipStream_t stream) {
  const u16* x = (const u16*)d_in[0]; const u16* wqkv = (const u16*)d_in[1]; const u16* wo = (const u16*)d_in[2];
  const u16* w_in = (const u16*)d_in[4]; const u16* w_sp = (const u16*)d_in[5]; const u16* b_sp = (const u16*)d_in[6]; const u16* w_out = (const u16*)d_in[7];
  char* ws = (char*)d_ws;
  u64* xch = (u64*)ws;
  u16* o = (u16*)(ws + (1 << 20)); u16* x1 = o + SEQ * DM; u16* x2 = x1 + SEQ * DM;
  u16* z1 = x2 + SEQ * DM; u16* z2n = z1 + SEQ * FH; u16* u = z2n + SEQ * 768;
  float* part = (float*)(ws + (512 << 10));
  const u16* xin = x;
  for (int L = 0; L < NLAYERS; ++L) {
    u16* xo = L == 0 ? x2 : (u16*)d_out;
    if (L == 0) k1_qkv_attn<2><<<256, 512, 0, stream>>>(xin, part, wqkv, o, xch, wo, L * 5 + 0);
    else k1_qkv_attn<1><<<256, 512, 0, stream>>>(xin, part, wqkv, o, nullptr, wo, L * 5 + 0);
    k_proj_res<8, DM, 0><<<256, 512, 0, stream>>>(o, wo, xin, x1, part, w_in, DM * FF * 2, L * 5 + 1);
    k3_win<<<256, 512, 0, stream>>>(x1, part, w_in, z1, z2n, xch + (size_t)L * 32 * 4 * 128, 0x5A5A0001u + L, w_sp, L * 5 + 2);
    k4_wsp<<<256, 512, 0, stream>>>(w_sp, z2n, z1, b_sp, u, w_out, L * 5 + 3);
    k_proj_res<11, UP, FH><<<256, 512, 0, stream>>>(u, w_out, x1, xo, part, L == 0 ? wqkv : nullptr, DM * 3 * DM * 2, L * 5 + 4);
    xin = xo;
  }
}
```

```cpp
#include <hip/hip_runtime.h>
#include <hip/hip_bf16.h>
#include <stdint.h>
#include <type_traits>

#define NLAYERS 2
typedef unsigned short u16;
typedef unsigned long long u64;
typedef __attribute__((ext_vector_type(8))) short bf16x8;
typedef __attribute__((ext_vector_type(4))) short s16x4;
typedef __attribute__((ext_vector_type(4))) float f32x4;
typedef __attribute__((ext_vector_type(2))) __bf16 bf16x2;
typedef __attribute__((ext_vector_type(4))) unsigned u32x4;
typedef __attribute__((ext_vector_type(2))) unsigned u32x2;
typedef __attribute__((address_space(3))) const char* lds_cptr;
typedef short v4i16_t __attribute__((ext_vector_type(4)));
#define DEV __device__ __forceinline__

constexpr int SEQ = 2048, DM = 512, FH = 688, FF = 1376, UP = 704;

template <int I, int N, class F> DEV void static_for(F&& f) {
  if constexpr (I < N) { f(std::integral_constant<int, I>{}); static_for<I + 1, N>(f); }
}
DEV void glds16(const void* g, unsigned lds_base) {
  unsigned sv;
  asm volatile("s_mov_b32 %0, m0\n\ts_mov_b32 m0, %2\n\ts_nop 0\n\tglobal_load_lds_dwordx4 %1, off\n\ts_mov_b32 m0, %0"
               : "=&s"(sv) : "v"(g), "s"(lds_base) : "memory");
}
template <int IMM> DEV void glds16s(unsigned voff, const void* sbase, unsigned ldsbase) {
  asm volatile("s_add_u32 m0, %1, %2\n\ts_nop 0\n\tglobal_load_lds_dwordx4 %0, %3" ::"v"(voff), "s"(ldsbase), "i"(IMM), "s"(sbase) : "memory", "scc");
}
template <int N> DEV void wait_bar() { asm volatile("s_waitcnt vmcnt(%0) lgkmcnt(0)\n\ts_barrier" ::"i"(N) : "memory"); }
DEV s16x4 trread(lds_cptr p) {
  return __builtin_bit_cast(s16x4, __builtin_amdgcn_ds_read_tr16_b64_v4i16((__attribute__((address_space(3))) v4i16_t*)p));
}
DEV float bf2f(unsigned v) { return __uint_as_float(v << 16); }
DEV unsigned cvtpk(float lo, float hi) { bf16x2 v = {(__bf16)lo, (__bf16)hi}; return __builtin_bit_cast(unsigned, v); }
DEV float rbf(float f) { return __uint_as_float(cvtpk(f, f) << 16); }
DEV float dot2(unsigned a, unsigned b, float c) {
  return __builtin_amdgcn_fdot2_f32_bf16(__builtin_bit_cast(bf16x2, a), __builtin_bit_cast(bf16x2, b), c, false);
}
template <int CTRL> DEV float dpp(float v) { return __int_as_float(__builtin_amdgcn_mov_dpp(__float_as_int(v), CTRL, 0xF, 0xF, true)); }
DEV float sum_x1(float v) { return v + dpp<0xB1>(v); }
DEV float sum_x2(float v) { return v + dpp<0x4E>(v); }
DEV float sum_x4(float v) { return v + dpp<0x141>(v); }
DEV float sum_g(float v) {
  auto a = __builtin_amdgcn_permlane16_swap(__float_as_uint(v), __float_as_uint(v), false, false);
  v = __uint_as_float(a[0]) + __uint_as_float(a[1]);
  auto b = __builtin_amdgcn_permlane32_swap(__float_as_uint(v), __float_as_uint(v), false, false);
  return __uint_as_float(b[0]) + __uint_as_float(b[1]);
}
DEV float max_g(float v) {
  auto a = __builtin_amdgcn_permlane16_swap(__float_as_uint(v), __float_as_uint(v), false, false);
  v = fmaxf(__uint_as_float(a[0]), __uint_as_float(a[1]));
  auto b = __builtin_amdgcn_permlane32_swap(__float_as_uint(v), __float_as_uint(v), false, false);
  return fmaxf(__uint_as_float(b[0]), __uint_as_float(b[1]));
}
#define SB0() __builtin_amdgcn_sched_barrier(0)
#define MFMA16(a, b, c) __builtin_amdgcn_mfma_f32_16x16x32_bf16(a, b, c, 0, 0, 0)

#define STAMP(KID, IDX) do { } while (0)
DEV void l2_touch(const void* p, int bytes, int lane, unsigned junk_lds) {
  return;
  const char* a = (const char*)p + lane * 128;
#pragma unroll 1
  for (int off = lane * 128; off < bytes; off += 8192, a += 8192) {
    unsigned sv;
    asm volatile("s_mov_b32 %0, m0\n\ts_mov_b32 m0, %2\n\ts_nop 0\n\tglobal_load_lds_dword %1, off\n\ts_mov_b32 m0, %0"
                 : "=&s"(sv) : "v"(a), "s"(junk_lds) : "memory");
  }
}
DEV void touch_always(const void* p, int bytes, int lane, unsigned junk_lds) {
  const char* a = (const char*)p + lane * 128;
#pragma unroll 1
  for (int off = lane * 128; off < bytes; off += 8192, a += 8192) {
    unsigned sv;
    asm volatile("s_mov_b32 %0, m0\n\ts_mov_b32 m0, %2\n\ts_nop 0\n\tglobal_load_lds_dword %1, off\n\ts_mov_b32 m0, %0"
                 : "=&s"(sv) : "v"(a), "s"(junk_lds) : "memory");
  }
}
template <int WRB> DEV int wswz(int k) {
  if constexpr (WRB % 256 == 128) return ((k >> 1) & 1) | (((k >> 3) & 1) << 1);
  else return (k >> 3) & 1;
}

template <int N> DEV void wait_vm() { asm volatile("s_waitcnt vmcnt(%0)" ::"i"(N) : "memory"); }
DEV void bar() { asm volatile("s_waitcnt lgkmcnt(0)\n\ts_barrier" ::: "memory"); }

template <int XR, int WRB, int NK, int S, int R, int MBW, int NBW, int LN, int KCLAMP, bool VT = false>
struct Core {
  static constexpr int XI = (XR * 2 + 63) / 64, WI = WRB / 64, LPS = XI + WI;
  static constexpr int XB = XR * 128, STG = XB + 64 * WRB, RING = S * STG, NR = (NK + R - 1) / R;
  static constexpr int JUNK = RING + XR * 8, FLG = JUNK + 256, LDS_BYTES = FLG + 32;
  static constexpr int D = 2;
  unsigned xo[XI], wo_[WI]; int wk[WI];
  const char* xb; const char* wb;
  unsigned ldsx, ldsw; int ldw; int lane;

  template <class FR, class FC>
  DEV void init(unsigned lds0, int wid, int lane_, const u16* X, int ldx, const u16* W, int ldw_, FR xrow, FC wcol) {
    lane = lane_; ldw = ldw_; xb = (const char*)X; wb = (const char*)W;
    ldsx = lds0 + wid * (XR * 32); ldsw = lds0 + XB + wid * (16 * WRB);
#pragma unroll
    for (int i = 0; i < XI; ++i) {
      int ci = i * 64 + lane; if (ci >= XR * 2) ci = XR * 2 - 1;
      int r = wid * (XR / 4) + (ci >> 3), pos = ci & 7;
      xo[i] = (unsigned)(xrow(r) * ldx + ((pos ^ (r & 7)) << 3)) * 2u;
    }
#pragma unroll
    for (int i = 0; i < WI; ++i) {
      int b = i * 1024 + lane * 16; int kr = wid * 16 + b / WRB, ir = b % WRB;
      int u = (ir >> 5) ^ wswz<WRB>(kr);
      wk[i] = kr;
      wo_[i] = (unsigned)(kr * ldw + wcol(u) + ((ir >> 4) & 1) * 8) * 2u;
    }
  }
  template <class P> DEV void issue(int st, unsigned sb, P&& poll) {
    const unsigned lx = ldsx + sb, lw = ldsw + sb;
    static_for<0, XI>([&](auto ic) {
      constexpr int i = ic.value;
      if constexpr ((XR * 2) % 64 != 0 && i == XI - 1) { if (lane < (XR * 2) % 64) glds16s<i * 1024>(xo[i], xb, lx); }
      else glds16s<i * 1024>(xo[i], xb, lx);
    });
    poll(st * LPS + XI);
    if constexpr (KCLAMP > 0) {
      if (st == NK - 1) {
#pragma unroll
        for (int i = 0; i < WI; ++i) { int over = st * 64 + wk[i] - (KCLAMP - 1); if (over > 0) wo_[i] -= (unsigned)(over * ldw) * 2u; }
      }
    }
    static_for<0, WI>([&](auto ic) {
      constexpr int i = ic.value;
      glds16s<i * 1024>(wo_[i], wb, lw);
      if constexpr (i % 3 == 2 || i == WI - 1) poll(st * LPS + XI + i + 1);
    });
    xb += 128; wb += (size_t)128 * ldw;
  }
  static DEV void wait_stages(int n) {
    static_for<0, S>([&](auto rc) { if (n == rc.value) wait_vm<rc.value * LPS>(); });
  }
  static DEV unsigned min4(const char* p) {
    u32x4 v; asm volatile("ds_read_b128 %0, %1\n\ts_waitcnt lgkmcnt(0)" : "=&v"(v) : "v"((unsigned)(uintptr_t)p) : "memory");
    unsigned m = min(min(v[0], v[1]), min(v[2], v[3]));
    return __builtin_amdgcn_readfirstlane(m);
  }
  static DEV void flag_store(unsigned addr, unsigned v) { asm volatile("ds_write_b32 %0, %1" :: "v"(addr), "v"(v) : "memory"); }
  static DEV void flags_init(char* lds) { if (threadIdx.x < 8) ((unsigned*)(lds + FLG))[threadIdx.x] = 0u; bar(); }
  DEV void load_loop(char* lds, int wid, int kid) {
    static_assert(S <= NK && D < S, "ring shape");
    const unsigned landed = (unsigned)(uintptr_t)(lds + FLG) + wid * 4;
    const char* done = lds + FLG + 16;
    unsigned sb = 0; int pub = 0;
    f32x4 s1[XI] = {}, s2[XI] = {};
    auto publish = [&](int n) {
      if constexpr (LN == 2) {
#pragma unroll 1
        for (int st = pub; st < n; ++st) {
          const unsigned cb = (unsigned)(st % S) * STG;
#pragma unroll
          for (int i = 0; i < XI; ++i) {
            u32x4 v = *(const u32x4*)(lds + cb + wid * (XR * 32) + i * 1024 + lane * 16);
#pragma unroll
            for (int j = 0; j < 4; ++j) {
              float lo = __uint_as_float(v[j] << 16), hi = __uint_as_float(v[j] & 0xFFFF0000u);
              s1[i][j] += lo + hi; s2[i][j] = fmaf(lo, lo, fmaf(hi, hi, s2[i][j]));
            }
          }
        }
      }
      if (n > pub) { pub = n; flag_store(landed, (unsigned)n); }
    };
    auto poll = [&](int ops) {
      unsigned x; asm volatile("s_getreg_b32 %0, hwreg(HW_REG_IB_STS)" : "=s"(x) :: "memory");
      const int vm = (int)((x & 15u) | ((x >> 18) & 48u));
      publish((ops - vm) / LPS);
    };
    unsigned dn = 0;
#pragma unroll 1
    for (int st = 0; st < NK; ++st) {
      if (st >= S) {
        const unsigned need = st - S + 1;
        while (dn < need) { dn = min4(done); poll(st * LPS); }
      }
      issue(st, sb, poll); sb += STG; if (sb == RING) sb = 0;
    }
    while (pub < NK) poll(NK * LPS);
    if constexpr (LN == 2) {
      float* st = (float*)(lds + RING);
#pragma unroll
      for (int i = 0; i < XI; ++i) {
        float a = (s1[i][0] + s1[i][1]) + (s1[i][2] + s1[i][3]), b = (s2[i][0] + s2[i][1]) + (s2[i][2] + s2[i][3]);
        a = sum_x4(sum_x2(sum_x1(a))); b = sum_x4(sum_x2(sum_x1(b)));
        float mean = a * (1.f / DM), var = fmaxf(b * (1.f / DM) - mean * mean, 0.f);
        int ci = i * 64 + lane;
        if ((lane & 7) == 0 && ci < XR * 2) { int r = wid * (XR / 4) + (ci >> 3); st[r * 2] = mean; st[r * 2 + 1] = rsqrtf(var + 1e-5f); }
      }
    }
    if constexpr (LN != 0) bar();
  }
  template <class FR> static DEV void stats_from(char* lds, int tid, const float* part, FR xrow) {
    float* st = (float*)(lds + RING);
#pragma unroll
    for (int r = tid >> 2; r < XR; r += 64) {
      const f32x4* p = (const f32x4*)(part + (size_t)xrow(r) * 32 + (tid & 3) * 8);
      f32x4 a = p[0], b = p[1];
      float s = (a[0] + a[2]) + (b[0] + b[2]), q = (a[1] + a[3]) + (b[1] + b[3]);
      s = sum_x2(sum_x1(s)); q = sum_x2(sum_x1(q));
      float mean = s * (1.f / DM), var = fmaxf(q * (1.f / DM) - mean * mean, 0.f);
      if ((tid & 3) == 0) { st[r * 2] = mean; st[r * 2 + 1] = rsqrtf(var + 1e-5f); }
    }
  }
  struct Frags { bf16x8 x[2][MBW], w[2][NBW]; };
  static DEV void run(char* lds, int wid, int lane, int xr0, const int (&wu)[NBW], f32x4 (&acc)[NBW][MBW], f32x4 (&cs)[NBW], int kid) {
    const int g = lane >> 4, q = (lane & 15) >> 2, p = lane & 3;
    lds_cptr L = (lds_cptr)lds;
    const int xa0 = (xr0 + (lane & 15)) * 128 + ((g ^ (lane & 7)) << 4);
    int wa[NBW];
#pragma unroll
    for (int nb = 0; nb < NBW; ++nb) wa[nb] = XB + (8 * g + q) * WRB + ((wu[nb] ^ wswz<WRB>(8 * g + q)) << 5) + p * 8;
    bf16x8 ones;
#pragma unroll
    for (int j = 0; j < 8; ++j) ones[j] = 0x3F80;
    int sb = 0;
    auto ld = [&](Frags& f) {
#pragma unroll
      for (int ks = 0; ks < 2; ++ks) {
#pragma unroll
        for (int nb = 0; nb < NBW; ++nb) {
          s16x4 lo = trread(L + sb + wa[nb] + (32 * ks) * WRB), hi = trread(L + sb + wa[nb] + (32 * ks + 4) * WRB);
          f.w[ks][nb] = __builtin_shufflevector(lo, hi, 0, 1, 2, 3, 4, 5, 6, 7);
        }
#pragma unroll
        for (int mb = 0; mb < MBW; ++mb)
          f.x[ks][mb] = *(const __attribute__((address_space(3))) bf16x8*)(L + sb + ((xa0 ^ (ks * 64)) + mb * 2048));
      }
      sb += STG; if (sb == RING) sb = 0;
    };
    auto mm = [&](const Frags& f) {
#pragma unroll
      for (int ks = 0; ks < 2; ++ks)
#pragma unroll
        for (int nb = 0; nb < NBW; ++nb) {
#pragma unroll
          for (int mb = 0; mb < MBW; ++mb) {
            if (VT && nb == NBW - 1) acc[nb][mb] = MFMA16(f.x[ks][mb], f.w[ks][nb], acc[nb][mb]);
            else acc[nb][mb] = MFMA16(f.w[ks][nb], f.x[ks][mb], acc[nb][mb]);
          }
          if constexpr (LN != 0) {
            if (VT && nb == NBW - 1) cs[nb] = MFMA16(ones, f.w[ks][nb], cs[nb]);
            else cs[nb] = MFMA16(f.w[ks][nb], ones, cs[nb]);
          }
        }
    };
    constexpr int NM = 2 * NBW * (MBW + (LN != 0 ? 1 : 0)), NRD = 2 * (2 * NBW + MBW), RPM = (NRD + NM - 1) / NM;
    auto step = [&](Frags& nx, const Frags& cu) {
      SB0(); ld(nx); mm(cu);
#pragma unroll
      for (int i = 0; i < NM; ++i) { __builtin_amdgcn_sched_group_barrier(0x008, 1, 0); __builtin_amdgcn_sched_group_barrier(0x100, RPM, 0); }
      SB0();
    };
    Frags A, B;
    const char* landed = lds + FLG;
    const unsigned done = (unsigned)(uintptr_t)(lds + FLG + 16) + wid * 4;
    unsigned fl = 0;
    auto sync = [&](int sdone, int need) {
      asm volatile("s_waitcnt lgkmcnt(0)" ::: "memory");
      flag_store(done, (unsigned)sdone);
      if (fl < (unsigned)need) { fl = min4(landed); while (fl < (unsigned)need) { __builtin_amdgcn_s_sleep(1); fl = min4(landed); } }
    };
    sync(0, 1); ld(A);
    if (wid == 0) STAMP(kid, 1);
#pragma unroll 1
    for (int s = 0; s + 2 < NK; s += 2) {
      sync(s + 1, s + 2); step(B, A);
      sync(s + 2, s + 3); step(A, B);
    }
    if constexpr (NK % 2 == 0) { sync(NK - 1, NK); step(B, A); mm(B); }
    else { mm(A); }
    if constexpr (LN != 0) bar();
  }
};

DEV void tile_of(int& rt, int& ct) { int b = blockIdx.x, xcd = b & 7, idx = b >> 3; rt = xcd * 4 + (idx & 3); ct = idx >> 2; }
DEV void warm_share(const void* p, int bytes, int w4, int lane, unsigned junk) { const int sh = bytes / 128; l2_touch((const char*)p + (size_t)((blockIdx.x >> 3) * 4 + w4) * sh, sh, lane, junk); }

DEV s16x4 pack4(f32x4 v) { u32x2 p = {cvtpk(v[0], v[1]), cvtpk(v[2], v[3])}; return __builtin_bit_cast(s16x4, p); }
#define MFMA16K16(a, b, c) __builtin_amdgcn_mfma_f32_16x16x16bf16_1k(a, b, c, 0, 0, 0)
template <int LNM>
__global__ __launch_bounds__(512, 2) void k1_qkv_attn(const u16* __restrict__ x, const float* __restrict__ part, const u16* __restrict__ wqkv, u16* __restrict__ o, u64* zero_buf, const u16* __restrict__ wnext, const u16* w2, const u16* w3, const u16* w4, int kid) {
  if (threadIdx.x == 0) STAMP(kid, 0);
  using C = Core<80, 384, 8, 4, 1, 5, 3, LNM, 0, true>;
  __shared__ __attribute__((aligned(16))) char lds[C::LDS_BYTES];
  const int tid = threadIdx.x, lane = tid & 63, wid = __builtin_amdgcn_readfirstlane(tid >> 6);
  int rt, ct; tile_of(rt, ct); const int r0 = rt * 64;
  C::flags_init(lds);
  if (zero_buf && tid < 128) zero_buf[blockIdx.x * 128 + tid] = 0ull;
  if (wid >= 4) {
    C c; c.init((unsigned)(uintptr_t)lds, wid - 4, lane, x, DM, wqkv, 3 * DM,
              [&](int r) { int gr = r0 - 16 + r; return gr < 0 ? 0 : gr; },
              [&](int u) { return (u >> 2) * DM + (ct * 4 + (u & 3)) * 16; });
    c.load_loop(lds, wid - 4, kid);
    if constexpr (LNM == 2) {
      const unsigned junk = (unsigned)(uintptr_t)lds + C::JUNK; const int part_ = blockIdx.x * 4 + wid - 4;
      touch_always((const char*)wnext + (size_t)part_ * 512, 512, lane, junk);
      if (part_ < 688) touch_always((const char*)w2 + (size_t)part_ * 2048, 2048, lane, junk);
      touch_always((const char*)w3 + (size_t)part_ * 8192, 8192, lane, junk);
      if (part_ < 688) touch_always((const char*)w4 + (size_t)part_ * 1024, 1024, lane, junk);
    }
    if (wid == 4) STAMP(kid, 2);
    return;
  }
  if constexpr (LNM == 1) C::stats_from(lds, tid, part, [&](int r) { int gr = r0 - 16 + r; return gr < 0 ? 0 : gr; });
  f32x4 acc[3][5] = {}, cs[3] = {};
  const int wu[3] = {wid, 4 + wid, 8 + wid};
  C::run(lds, wid, lane, 0, wu, acc, cs, kid); if (wid == 0) STAMP(kid, 3);
  const float* stt = (const float*)(lds + C::RING);
  const int g = lane >> 4, m16 = lane & 15;
  s16x4 qf[5], kf[5], vf[5];
#pragma unroll
  for (int mb = 0; mb < 5; ++mb) {
    const float rs = stt[(mb * 16 + m16) * 2 + 1], cm = -rs * stt[(mb * 16 + m16) * 2];
    if (mb > 0) qf[mb] = pack4(rs * acc[0][mb] + cm * cs[0]);
    kf[mb] = pack4(rs * acc[1][mb] + cm * cs[1]);
    const f32x4 sa = *(const f32x4*)(stt + (mb * 16 + 4 * g) * 2), sb2 = *(const f32x4*)(stt + (mb * 16 + 4 * g) * 2 + 4);
    f32x4 v = {sa[1] * (acc[2][mb][0] - sa[0] * cs[2][0]), sa[3] * (acc[2][mb][1] - sa[2] * cs[2][0]),
               sb2[1] * (acc[2][mb][2] - sb2[0] * cs[2][0]), sb2[3] * (acc[2][mb][3] - sb2[2] * cs[2][0])};
    vf[mb] = pack4(v);
  }
  bool v0[4], v1[4];
#pragma unroll
  for (int j = 0; j < 4; ++j) { int d0 = m16 + 16 - (4 * g + j), d1 = m16 - (4 * g + j); v0[j] = d0 <= 14 && !(d0 & 1); v1[j] = d1 >= 0 && d1 <= 14 && !(d1 & 1); }
  const float sc = 0.25f * 1.4426950408889634f;
#pragma unroll
  for (int qb = 0; qb < 4; ++qb) {
    f32x4 s0 = MFMA16K16(kf[qb], qf[qb + 1], (f32x4{})), s1 = MFMA16K16(kf[qb + 1], qf[qb + 1], (f32x4{}));
    const bool prev_ok = (r0 + qb * 16) > 0;
    float mx = -3.0e38f;
#pragma unroll
    for (int j = 0; j < 4; ++j) {
      s0[j] = (v0[j] && prev_ok) ? s0[j] * sc : -1.0e9f; s1[j] = v1[j] ? s1[j] * sc : -1.0e9f;
      mx = fmaxf(mx, fmaxf(s0[j], s1[j]));
    }
    mx = max_g(mx);
    float sum = 0.f;
#pragma unroll
    for (int j = 0; j < 4; ++j) { s0[j] = __builtin_amdgcn_exp2f(s0[j] - mx); s1[j] = __builtin_amdgcn_exp2f(s1[j] - mx); sum += s0[j] + s1[j]; }
    sum = sum_g(sum);
    const float inv = __builtin_amdgcn_rcpf(sum);
    f32x4 ot = MFMA16K16(vf[qb], pack4(s0 * inv), (f32x4{}));
    ot = MFMA16K16(vf[qb + 1], pack4(s1 * inv), ot);
    *(u32x2*)(o + (size_t)(r0 + qb * 16 + m16) * DM + (ct * 4 + wid) * 16 + 4 * g) = u32x2{cvtpk(ot[0], ot[1]), cvtpk(ot[2], ot[3])};
  }
  if (wid == 0) STAMP(kid, 4);
}

template <int NK, int LDA, int KCLAMP>
__global__ __launch_bounds__(512, 2) void k_proj_res(const u16* __restrict__ a, const u16* __restrict__ w, const u16* __restrict__ res, u16* __restrict__ out, float* __restrict__ part, const u16* __restrict__ wnext, int wnext_bytes, int kid) {
  if (threadIdx.x == 0) STAMP(kid, 0);
  using C = Core<64, 128, NK, 8, 2, 2, 2, 0, KCLAMP>;
  __shared__ __attribute__((aligned(16))) char lds[C::LDS_BYTES];
  const int tid = threadIdx.x, lane = tid & 63, wid = __builtin_amdgcn_readfirstlane(tid >> 6);
  int rt, ct; tile_of(rt, ct); const int r0 = rt * 64, c0 = ct * 64;
  C::flags_init(lds);
  if (wid >= 4) {
    C c; c.init((unsigned)(uintptr_t)lds, wid - 4, lane, a, LDA, w, DM, [&](int r) { return r0 + r; }, [&](int u) { return c0 + u * 16; });
    c.load_loop(lds, wid - 4, kid);
    if (wnext) warm_share(wnext, wnext_bytes, wid - 4, lane, (unsigned)(uintptr_t)lds + C::JUNK); if (wid == 4) STAMP(kid, 2); return;
  }
  const int wm = wid >> 1, wn = wid & 1, g = lane >> 4, m16 = lane & 15;
  u32x2 rv[2][2];
#pragma unroll
  for (int nb = 0; nb < 2; ++nb)
#pragma unroll
    for (int mb = 0; mb < 2; ++mb) rv[nb][mb] = *(const u32x2*)(res + (size_t)(r0 + wm * 32 + mb * 16 + m16) * DM + c0 + (wn * 2 + nb) * 16 + g * 4);
  f32x4 acc[2][2] = {}, cs[2];
  const int wu[2] = {wn * 2, wn * 2 + 1};
  C::run(lds, wid, lane, wm * 32, wu, acc, cs, kid); if (wid == 0) STAMP(kid, 3);
#pragma unroll
  for (int mb = 0; mb < 2; ++mb) {
    float ps = 0.f, pq = 0.f;
#pragma unroll
    for (int nb = 0; nb < 2; ++nb) {
      f32x4 A = acc[nb][mb]; u32x2 r = rv[nb][mb];
      float v0 = bf2f(r[0] & 0xFFFF) + rbf(A[0]), v1 = bf2f(r[0] >> 16) + rbf(A[1]), v2 = bf2f(r[1] & 0xFFFF) + rbf(A[2]), v3 = bf2f(r[1] >> 16) + rbf(A[3]);
      u32x2 o = {cvtpk(v0, v1), cvtpk(v2, v3)};
      *(u32x2*)(out + (size_t)(r0 + wm * 32 + mb * 16 + m16) * DM + c0 + (wn * 2 + nb) * 16 + g * 4) = o;
      v0 = bf2f(o[0] & 0xFFFF); v1 = bf2f(o[0] >> 16); v2 = bf2f(o[1] & 0xFFFF); v3 = bf2f(o[1] >> 16);
      ps += (v0 + v1) + (v2 + v3); pq += (v0 * v0 + v1 * v1) + (v2 * v2 + v3 * v3);
    }
    ps = sum_g(ps); pq = sum_g(pq);
    if (g == 0) *(float2*)(part + (size_t)(r0 + wm * 32 + mb * 16 + m16) * 32 + (ct * 2 + wn) * 2) = make_float2(ps, pq);
  }
  if (wid == 0) STAMP(kid, 4);
}

typedef __attribute__((ext_vector_type(2))) float f32x2;
DEV f32x2 gelu2(f32x2 t) {
  const float c1 = -1.5957691216057308f * 1.4426950408889634f, c2 = c1 * 0.044715f;
  f32x2 y = t * (t * t * c2 + c1);
  f32x2 d = {__builtin_amdgcn_exp2f(y[0]), __builtin_amdgcn_exp2f(y[1])};
  d += 1.f;
  f32x2 r = {__builtin_amdgcn_rcpf(d[0]), __builtin_amdgcn_rcpf(d[1])};
  return t * r;
}
__global__ __launch_bounds__(512, 2) void k3_win(const u16* __restrict__ x, const float* __restrict__ part, const u16* __restrict__ w_in, u16* __restrict__ z1, u16* __restrict__ z2n, u64* xch, unsigned tag, const u16* __restrict__ w_sp, int kid) {
  if (threadIdx.x == 0) STAMP(kid, 0);
  using C = Core<64, 384, 8, 4, 1, 4, 3, 1, 0>;
  __shared__ __attribute__((aligned(16))) char lds[C::LDS_BYTES];
  const int tid = threadIdx.x, lane = tid & 63, wid = __builtin_amdgcn_readfirstlane(tid >> 6), w4 = wid & 3;
  int rt, ct; tile_of(rt, ct); const int r0 = rt * 64;
  C::flags_init(lds);
  const int half = ct >> 2, zt = ct & 3, nun = zt == 3 ? 10 : 11, f0 = zt * 176;
  f32x4 a2[3][2], cs[3];
  f32x4* ho = (f32x4*)(lds + 4096) + w4 * (9 * 64) + lane;
  if (wid >= 4) {
    C c; c.init((unsigned)(uintptr_t)lds, wid - 4, lane, x, DM, w_in, FF, [&](int r) { return r0 + r; },
              [&](int u) { return half * FH + f0 + (u < nun ? u : 0) * 16; });
    c.load_loop(lds, wid - 4, kid);
    if (wid == 4) STAMP(kid, 2);
    bar();
#pragma unroll
    for (int nb = 0; nb < 3; ++nb) { a2[nb][0] = ho[(nb * 3) * 64]; a2[nb][1] = ho[(nb * 3 + 1) * 64]; cs[nb] = ho[(nb * 3 + 2) * 64]; }
  } else {
    C::stats_from(lds, tid, part, [&](int r) { return r0 + r; });
    f32x4 acc[3][4] = {};
#pragma unroll
    for (int nb = 0; nb < 3; ++nb) cs[nb] = f32x4{};
    const int wu[3] = {wid * 3, wid * 3 + 1, wid * 3 + 2};
    C::run(lds, wid, lane, 0, wu, acc, cs, kid); if (wid == 0) STAMP(kid, 3);
#pragma unroll
    for (int nb = 0; nb < 3; ++nb) { ho[(nb * 3) * 64] = acc[nb][2]; ho[(nb * 3 + 1) * 64] = acc[nb][3]; ho[(nb * 3 + 2) * 64] = cs[nb]; a2[nb][0] = acc[nb][0]; a2[nb][1] = acc[nb][1]; }
    bar();
  }
  const float* stt = (const float*)(lds + C::RING);
  const int g = lane >> 4, m16 = lane & 15, rb = (wid >> 2) * 32;
  u32x2 pk[3][2]; float p1[2], p2[2];
#pragma unroll
  for (int mb = 0; mb < 2; ++mb) {
    const float rs = stt[(rb + mb * 16 + m16) * 2 + 1], cm = -rs * stt[(rb + mb * 16 + m16) * 2];
    f32x2 s1 = {0.f, 0.f}, s2 = {0.f, 0.f};
#pragma unroll
    for (int nb = 0; nb < 3; ++nb) {
      f32x4 t = rs * a2[nb][mb] + cm * cs[nb];
      f32x2 lo = gelu2(f32x2{t[0], t[1]}), hi = gelu2(f32x2{t[2], t[3]});
      pk[nb][mb] = u32x2{cvtpk(lo[0], lo[1]), cvtpk(hi[0], hi[1])};
      if (w4 * 3 + nb < nun) {
        f32x2 a = {bf2f(pk[nb][mb][0] & 0xFFFF), bf2f(pk[nb][mb][0] >> 16)}, b = {bf2f(pk[nb][mb][1] & 0xFFFF), bf2f(pk[nb][mb][1] >> 16)};
        s1 += a + b; s2 += a * a + b * b;
      }
    }
    p1[mb] = s1[0] + s1[1]; p2[mb] = s2[0] + s2[1];
  }
  if (half == 0) {
#pragma unroll
    for (int mb = 0; mb < 2; ++mb)
#pragma unroll
      for (int nb = 0; nb < 3; ++nb)
        if (w4 * 3 + nb < nun) *(u32x2*)(z1 + (size_t)(r0 + rb + mb * 16 + m16) * FH + f0 + (w4 * 3 + nb) * 16 + g * 4) = pk[nb][mb];
    if (wid == 0) STAMP(kid, 4);
    return;
  }
  if (wid == 0) STAMP(kid, 5);
  float* red = (float*)lds;
  float* tot = red + 512;
#pragma unroll
  for (int mb = 0; mb < 2; ++mb) {
    float a = p1[mb], b = p2[mb];
    a = sum_g(a); b = sum_g(b);
    if (g == 0) *(float2*)(red + (w4 * 64 + rb + mb * 16 + m16) * 2) = make_float2(a, b);
  }
  __syncthreads();
  if (tid < 128) {
    float own = (red[tid] + red[128 + tid]) + (red[256 + tid] + red[384 + tid]);
    u64* base = xch + (size_t)rt * 4 * 128;
    __hip_atomic_store(base + zt * 128 + tid, ((u64)tag << 32) | __float_as_uint(own), __ATOMIC_RELAXED, __HIP_MEMORY_SCOPE_AGENT);
    u64 v[4]; int spins = 0; bool ok;
    do {
#pragma unroll
      for (int t = 0; t < 4; ++t) v[t] = __hip_atomic_load(base + t * 128 + tid, __ATOMIC_RELAXED, __HIP_MEMORY_SCOPE_AGENT);
      ok = true;
#pragma unroll
      for (int t = 0; t < 4; ++t) ok = ok && (t == zt || (unsigned)(v[t] >> 32) == tag);
    } while (!ok && ++spins < (1 << 22));
    float pt[4];
#pragma unroll
    for (int t = 0; t < 4; ++t) pt[t] = t == zt ? own : __uint_as_float((unsigned)v[t]);
    tot[tid] = (pt[0] + pt[1]) + (pt[2] + pt[3]);
  }
  __syncthreads();
  if (wid == 0) STAMP(kid, 6);
#pragma unroll
  for (int mb = 0; mb < 2; ++mb) {
    const int r = rb + mb * 16 + m16;
    const float mean = tot[r * 2] * (1.f / FH), var = fmaxf(tot[r * 2 + 1] * (1.f / FH) - mean * mean, 0.f);
    const float rs = rsqrtf(var + 1e-5f), cm = -mean * rs;
#pragma unroll
    for (int nb = 0; nb < 3; ++nb) {
      const int f = f0 + (w4 * 3 + nb) * 16;
      if (w4 * 3 + nb < nun) {
        u32x2 v = pk[nb][mb];
        *(u32x2*)(z2n + ((size_t)(f / 96) * SEQ + r0 + r) * 96 + f % 96 + g * 4) =
            u32x2{cvtpk(fmaf(bf2f(v[0] & 0xFFFF), rs, cm), fmaf(bf2f(v[0] >> 16), rs, cm)), cvtpk(fmaf(bf2f(v[1] & 0xFFFF), rs, cm), fmaf(bf2f(v[1] >> 16), rs, cm))};
      }
    }
  }
  if (wid == 0) STAMP(kid, 4);
}

__global__ __launch_bounds__(512, 2) void k4_wsp(const u16* __restrict__ w_sp, const u16* __restrict__ z2n, const u16* __restrict__ z1, const u16* __restrict__ b_sp, u16* __restrict__ u, const u16* __restrict__ wnext, int kid) {
  if (threadIdx.x == 0) STAMP(kid, 0);
  using C = Core<64, 192, 32, 7, 2, 2, 3, 0, 0>;
  __shared__ __attribute__((aligned(16))) char lds[C::LDS_BYTES];
  const int tid = threadIdx.x, lane = tid & 63, wid = __builtin_amdgcn_readfirstlane(tid >> 6);
  int rt, ct; tile_of(rt, ct); const int r0 = rt * 64, c0 = ct * 96;
  C::flags_init(lds);
  if (wid >= 4) {
    C c; c.init((unsigned)(uintptr_t)lds, wid - 4, lane, w_sp, SEQ, z2n + (size_t)ct * SEQ * 96, 96, [&](int r) { return r0 + r; },
              [&](int uu) { return uu * 16; });
    c.load_loop(lds, wid - 4, kid);
    warm_share(wnext, FH * DM * 2, wid - 4, lane, (unsigned)(uintptr_t)lds + C::JUNK); if (wid == 4) STAMP(kid, 2); return;
  }
  const int wm = wid >> 1, wn = wid & 1, g = lane >> 4, m16 = lane & 15;
  u32x2 zv[3][2]; float bv[2];
#pragma unroll
  for (int mb = 0; mb < 2; ++mb) {
    bv[mb] = bf2f(b_sp[r0 + wm * 32 + mb * 16 + m16]);
#pragma unroll
    for (int nb = 0; nb < 3; ++nb) {
      int col = c0 + (wn * 3 + nb) * 16; if (col >= FH) col = 0;
      zv[nb][mb] = *(const u32x2*)(z1 + (size_t)(r0 + wm * 32 + mb * 16 + m16) * FH + col + g * 4);
    }
  }
  f32x4 acc[3][2] = {}, cs[3];
  const int wu[3] = {wn * 3, wn * 3 + 1, wn * 3 + 2};
  C::run(lds, wid, lane, wm * 32, wu, acc, cs, kid); if (wid == 0) STAMP(kid, 3);
#pragma unroll
  for (int nb = 0; nb < 3; ++nb) {
    const int col = c0 + (wn * 3 + nb) * 16;
#pragma unroll
    for (int mb = 0; mb < 2; ++mb) {
      f32x4 A = acc[nb][mb]; u32x2 z = zv[nb][mb]; float b = bv[mb];
      float v0 = bf2f(z[0] & 0xFFFF) * rbf(rbf(A[0]) + b), v1 = bf2f(z[0] >> 16) * rbf(rbf(A[1]) + b);
      float v2 = bf2f(z[1] & 0xFFFF) * rbf(rbf(A[2]) + b), v3 = bf2f(z[1] >> 16) * rbf(rbf(A[3]) + b);
      u32x2 r = {cvtpk(v0, v1), cvtpk(v2, v3)};
      if (col >= FH) r = u32x2{0u, 0u};
      if (col < UP) *(u32x2*)(u + (size_t)(r0 + wm * 32 + mb * 16 + m16) * UP + col + g * 4) = r;
    }
  }
  if (wid == 0) STAMP(kid, 4);
}

extern "C" void kernel_launch(void* const* d_in, const int* in_sizes, int n_in, void* d_out, int out_size, void* d_ws, size_t ws_size, hipStream_t stream) {
  const u16* x = (const u16*)d_in[0]; const u16* wqkv = (const u16*)d_in[1]; const u16* wo = (const u16*)d_in[2];
  const u16* w_in = (const u16*)d_in[4]; const u16* w_sp = (const u16*)d_in[5]; const u16* b_sp = (const u16*)d_in[6]; const u16* w_out = (const u16*)d_in[7];
  char* ws = (char*)d_ws;
  u64* xch = (u64*)ws;
  u16* o = (u16*)(ws + (1 << 20)); u16* x1 = o + SEQ * DM; u16* x2 = x1 + SEQ * DM;
  u16* z1 = x2 + SEQ * DM; u16* z2n = z1 + SEQ * FH; u16* u = z2n + SEQ * 768;
  float* part = (float*)(ws + (512 << 10));
  const u16* xin = x;
  for (int L = 0; L < NLAYERS; ++L) {
    u16* xo = L == 0 ? x2 : (u16*)d_out;
    if (L == 0) k1_qkv_attn<2><<<256, 512, 0, stream>>>(xin, part, wqkv, o, xch, wo, w_in, w_sp, w_out, L * 5 + 0);
    else k1_qkv_attn<1><<<256, 512, 0, stream>>>(xin, part, wqkv, o, nullptr, wo, w_in, w_sp, w_out, L * 5 + 0);
    k_proj_res<8, DM, 0><<<256, 512, 0, stream>>>(o, wo, xin, x1, part, w_in, DM * FF * 2, L * 5 + 1);
    k3_win<<<256, 512, 0, stream>>>(x1, part, w_in, z1, z2n, xch + (size_t)L * 32 * 4 * 128, 0x5A5A0001u + L, w_sp, L * 5 + 2);
    k4_wsp<<<256, 512, 0, stream>>>(w_sp, z2n, z1, b_sp, u, w_out, L * 5 + 3);
    k_proj_res<11, UP, FH><<<256, 512, 0, stream>>>(u, w_out, x1, xo, part, L == 0 ? wqkv : nullptr, DM * 3 * DM * 2, L * 5 + 4);
    xin = xo;
  }
}
```

```cpp
#define EARLY_WARM
#define CODE_WARM
#include <hip/hip_runtime.h>
#include <hip/hip_bf16.h>
#include <stdint.h>
#include <type_traits>

#define NLAYERS 2
typedef unsigned short u16;
typedef unsigned long long u64;
#define CW_K1 9600
#define CW_K2 3840
#define CW_K3 9344
#define CW_K4 4992
#define CW_K5 3584
typedef __attribute__((ext_vector_type(8))) short bf16x8;
typedef __attribute__((ext_vector_type(4))) short s16x4;
typedef __attribute__((ext_vector_type(4))) float f32x4;
typedef __attribute__((ext_vector_type(2))) __bf16 bf16x2;
typedef __attribute__((ext_vector_type(4))) unsigned u32x4;
typedef __attribute__((ext_vector_type(2))) unsigned u32x2;
typedef __attribute__((address_space(3))) const char* lds_cptr;
typedef short v4i16_t __attribute__((ext_vector_type(4)));
#define DEV __device__ __forceinline__

constexpr int SEQ = 2048, DM = 512, FH = 688, FF = 1376, UP = 704;

template <int I, int N, class F> DEV void static_for(F&& f) {
  if constexpr (I < N) { f(std::integral_constant<int, I>{}); static_for<I + 1, N>(f); }
}
DEV void glds16(const void* g, unsigned lds_base) {
  unsigned sv;
  asm volatile("s_mov_b32 %0, m0\n\ts_mov_b32 m0, %2\n\ts_nop 0\n\tglobal_load_lds_dwordx4 %1, off\n\ts_mov_b32 m0, %0"
               : "=&s"(sv) : "v"(g), "s"(lds_base) : "memory");
}
template <int IMM> DEV void glds16s(unsigned voff, const void* sbase, unsigned ldsbase) {
  asm volatile("s_add_u32 m0, %1, %2\n\ts_nop 0\n\tglobal_load_lds_dwordx4 %0, %3" ::"v"(voff), "s"(ldsbase), "i"(IMM), "s"(sbase) : "memory", "scc");
}
template <int N> DEV void wait_bar() { asm volatile("s_waitcnt vmcnt(%0) lgkmcnt(0)\n\ts_barrier" ::"i"(N) : "memory"); }
DEV s16x4 trread(lds_cptr p) {
  return __builtin_bit_cast(s16x4, __builtin_amdgcn_ds_read_tr16_b64_v4i16((__attribute__((address_space(3))) v4i16_t*)p));
}
DEV float bf2f(unsigned v) { return __uint_as_float(v << 16); }
DEV unsigned cvtpk(float lo, float hi) { bf16x2 v = {(__bf16)lo, (__bf16)hi}; return __builtin_bit_cast(unsigned, v); }
DEV float rbf(float f) { return __uint_as_float(cvtpk(f, f) << 16); }
DEV float dot2(unsigned a, unsigned b, float c) {
  return __builtin_amdgcn_fdot2_f32_bf16(__builtin_bit_cast(bf16x2, a), __builtin_bit_cast(bf16x2, b), c, false);
}
template <int CTRL> DEV float dpp(float v) { return __int_as_float(__builtin_amdgcn_mov_dpp(__float_as_int(v), CTRL, 0xF, 0xF, true)); }
DEV float sum_x1(float v) { return v + dpp<0xB1>(v); }
DEV float sum_x2(float v) { return v + dpp<0x4E>(v); }
DEV float sum_x4(float v) { return v + dpp<0x141>(v); }
DEV float sum_g(float v) {
  auto a = __builtin_amdgcn_permlane16_swap(__float_as_uint(v), __float_as_uint(v), false, false);
  v = __uint_as_float(a[0]) + __uint_as_float(a[1]);
  auto b = __builtin_amdgcn_permlane32_swap(__float_as_uint(v), __float_as_uint(v), false, false);
  return __uint_as_float(b[0]) + __uint_as_float(b[1]);
}
DEV float max_g(float v) {
  auto a = __builtin_amdgcn_permlane16_swap(__float_as_uint(v), __float_as_uint(v), false, false);
  v = fmaxf(__uint_as_float(a[0]), __uint_as_float(a[1]));
  auto b = __builtin_amdgcn_permlane32_swap(__float_as_uint(v), __float_as_uint(v), false, false);
  return fmaxf(__uint_as_float(b[0]), __uint_as_float(b[1]));
}
#define SB0() __builtin_amdgcn_sched_barrier(0)
#define MFMA16(a, b, c) __builtin_amdgcn_mfma_f32_16x16x32_bf16(a, b, c, 0, 0, 0)

#define STAMP(KID, IDX) do { } while (0)
DEV void l2_touch(const void* p, int bytes, int lane, unsigned junk_lds) {
  return;
  const char* a = (const char*)p + lane * 128;
#pragma unroll 1
  for (int off = lane * 128; off < bytes; off += 8192, a += 8192) {
    unsigned sv;
    asm volatile("s_mov_b32 %0, m0\n\ts_mov_b32 m0, %2\n\ts_nop 0\n\tglobal_load_lds_dword %1, off\n\ts_mov_b32 m0, %0"
                 : "=&s"(sv) : "v"(a), "s"(junk_lds) : "memory");
  }
}
DEV void touch_always(const void* p, int bytes, int lane, unsigned junk_lds) {
  const char* a = (const char*)p + lane * 128;
#pragma unroll 1
  for (int off = lane * 128; off < bytes; off += 8192, a += 8192) {
    unsigned sv;
    asm volatile("s_mov_b32 %0, m0\n\ts_mov_b32 m0, %2\n\ts_nop 0\n\tglobal_load_lds_dword %1, off\n\ts_mov_b32 m0, %0"
                 : "=&s"(sv) : "v"(a), "s"(junk_lds) : "memory");
  }
}
template <int WRB> DEV int wswz(int k) {
  if constexpr (WRB % 256 == 128) return ((k >> 1) & 1) | (((k >> 3) & 1) << 1);
  else return (k >> 3) & 1;
}

template <int N> DEV void wait_vm() { asm volatile("s_waitcnt vmcnt(%0)" ::"i"(N) : "memory"); }
DEV void bar() { asm volatile("s_waitcnt lgkmcnt(0)\n\ts_barrier" ::: "memory"); }

template <int XR, int WRB, int NK, int S, int R, int MBW, int NBW, int LN, int KCLAMP, bool VT = false>
struct Core {
  static constexpr int XI = (XR * 2 + 63) / 64, WI = WRB / 64, LPS = XI + WI;
  static constexpr int XB = XR * 128, STG = XB + 64 * WRB, RING = S * STG, NR = (NK + R - 1) / R;
  static constexpr int JUNK = RING + XR * 8, FLG = JUNK + 256, LDS_BYTES = FLG + 32;
  static constexpr int D = 2;
  unsigned xo[XI], wo_[WI]; int wk[WI];
  const char* xb; const char* wb;
  unsigned ldsx, ldsw; int ldw; int lane;

  template <class FR, class FC>
  DEV void init(unsigned lds0, int wid, int lane_, const u16* X, int ldx, const u16* W, int ldw_, FR xrow, FC wcol) {
    lane = lane_; ldw = ldw_; xb = (const char*)X; wb = (const char*)W;
    ldsx = lds0 + wid * (XR * 32); ldsw = lds0 + XB + wid * (16 * WRB);
#pragma unroll
    for (int i = 0; i < XI; ++i) {
      int ci = i * 64 + lane; if (ci >= XR * 2) ci = XR * 2 - 1;
      int r = wid * (XR / 4) + (ci >> 3), pos = ci & 7;
      xo[i] = (unsigned)(xrow(r) * ldx + ((pos ^ (r & 7)) << 3)) * 2u;
    }
#pragma unroll
    for (int i = 0; i < WI; ++i) {
      int b = i * 1024 + lane * 16; int kr = wid * 16 + b / WRB, ir = b % WRB;
      int u = (ir >> 5) ^ wswz<WRB>(kr);
      wk[i] = kr;
      wo_[i] = (unsigned)(kr * ldw + wcol(u) + ((ir >> 4) & 1) * 8) * 2u;
    }
  }
  template <class P> DEV void issue(int st, unsigned sb, P&& poll) {
    const unsigned lx = ldsx + sb, lw = ldsw + sb;
    static_for<0, XI>([&](auto ic) {
      constexpr int i = ic.value;
      if constexpr ((XR * 2) % 64 != 0 && i == XI - 1) { if (lane < (XR * 2) % 64) glds16s<i * 1024>(xo[i], xb, lx); }
      else glds16s<i * 1024>(xo[i], xb, lx);
    });
    poll(st * LPS + XI - 1);
    if constexpr (KCLAMP > 0) {
      if (st == NK - 1) {
#pragma unroll
        for (int i = 0; i < WI; ++i) { int over = st * 64 + wk[i] - (KCLAMP - 1); if (over > 0) wo_[i] -= (unsigned)(over * ldw) * 2u; }
      }
    }
    static_for<0, WI>([&](auto ic) {
      constexpr int i = ic.value;
      glds16s<i * 1024>(wo_[i], wb, lw);
      if constexpr (i % 3 == 2 || i == WI - 1) poll(st * LPS + XI + i);
    });
    xb += 128; wb += (size_t)128 * ldw;
  }
  static DEV void wait_stages(int n) {
    static_for<0, S>([&](auto rc) { if (n == rc.value) wait_vm<rc.value * LPS>(); });
  }
  static DEV unsigned min4(const char* p) {
    u32x4 v; asm volatile("ds_read_b128 %0, %1\n\ts_waitcnt lgkmcnt(0)" : "=&v"(v) : "v"((unsigned)(uintptr_t)p) : "memory");
    unsigned m = min(min(v[0], v[1]), min(v[2], v[3]));
    return __builtin_amdgcn_readfirstlane(m);
  }
  static DEV void flag_store(unsigned addr, unsigned v) { asm volatile("ds_write_b32 %0, %1" :: "v"(addr), "v"(v) : "memory"); }
  static DEV void flags_init(char* lds) { if (threadIdx.x < 8) ((unsigned*)(lds + FLG))[threadIdx.x] = 0u; bar(); }
  DEV void load_loop(char* lds, int wid, int kid) {
    static_assert(S <= NK && D < S, "ring shape");
    const unsigned landed = (unsigned)(uintptr_t)(lds + FLG) + wid * 4;
    const char* done = lds + FLG + 16;
    unsigned sb = 0; int pub = 0;
    f32x4 s1[XI] = {}, s2[XI] = {};
    auto publish = [&](int n) {
      if constexpr (LN == 2) {
#pragma unroll 1
        for (int st = pub; st < n; ++st) {
          const unsigned cb = (unsigned)(st % S) * STG;
#pragma unroll
          for (int i = 0; i < XI; ++i) {
            u32x4 v = *(const u32x4*)(lds + cb + wid * (XR * 32) + i * 1024 + lane * 16);
#pragma unroll
            for (int j = 0; j < 4; ++j) {
              float lo = __uint_as_float(v[j] << 16), hi = __uint_as_float(v[j] & 0xFFFF0000u);
              s1[i][j] += lo + hi; s2[i][j] = fmaf(lo, lo, fmaf(hi, hi, s2[i][j]));
            }
          }
        }
      }
      if (n > pub) { pub = n; flag_store(landed, (unsigned)n); }
    };
    auto poll = [&](int ops) {
      unsigned x; asm volatile("s_nop 7\n\ts_getreg_b32 %0, hwreg(HW_REG_IB_STS)" : "=s"(x) :: "memory");
      const int vm = (int)((x & 15u) | ((x >> 18) & 48u));
      if (ops > vm) publish((ops - vm) / LPS);
    };
    unsigned dn = 0;
#pragma unroll 1
    for (int st = 0; st < NK; ++st) {
      if (st >= S) {
        const unsigned need = st - S + 1;
        while (dn < need) { dn = min4(done); poll(st * LPS); }
      }
      issue(st, sb, poll); sb += STG; if (sb == RING) sb = 0;
    }
    while (pub < NK) poll(NK * LPS);
    if constexpr (LN == 2) {
      float* st = (float*)(lds + RING);
#pragma unroll
      for (int i = 0; i < XI; ++i) {
        float a = (s1[i][0] + s1[i][1]) + (s1[i][2] + s1[i][3]), b = (s2[i][0] + s2[i][1]) + (s2[i][2] + s2[i][3]);
        a = sum_x4(sum_x2(sum_x1(a))); b = sum_x4(sum_x2(sum_x1(b)));
        float mean = a * (1.f / DM), var = fmaxf(b * (1.f / DM) - mean * mean, 0.f);
        int ci = i * 64 + lane;
        if ((lane & 7) == 0 && ci < XR * 2) { int r = wid * (XR / 4) + (ci >> 3); st[r * 2] = mean; st[r * 2 + 1] = rsqrtf(var + 1e-5f); }
      }
    }
    if constexpr (LN != 0) bar();
  }
  template <class FR> static DEV void stats_from(char* lds, int tid, const float* part, FR xrow) {
    float* st = (float*)(lds + RING);
#pragma unroll
    for (int r = tid >> 2; r < XR; r += 64) {
      const f32x4* p = (const f32x4*)(part + (size_t)xrow(r) * 32 + (tid & 3) * 8);
      f32x4 a = p[0], b = p[1];
      float s = (a[0] + a[2]) + (b[0] + b[2]), q = (a[1] + a[3]) + (b[1] + b[3]);
      s = sum_x2(sum_x1(s)); q = sum_x2(sum_x1(q));
      float mean = s * (1.f / DM), var = fmaxf(q * (1.f / DM) - mean * mean, 0.f);
      if ((tid & 3) == 0) { st[r * 2] = mean; st[r * 2 + 1] = rsqrtf(var + 1e-5f); }
    }
  }
  struct Frags { bf16x8 x[2][MBW], w[2][NBW]; };
  static DEV void run(char* lds, int wid, int lane, int xr0, const int (&wu)[NBW], f32x4 (&acc)[NBW][MBW], f32x4 (&cs)[NBW], int kid) {
    const int g = lane >> 4, q = (lane & 15) >> 2, p = lane & 3;
    lds_cptr L = (lds_cptr)lds;
    const int xa0 = (xr0 + (lane & 15)) * 128 + ((g ^ (lane & 7)) << 4);
    int wa[NBW];
#pragma unroll
    for (int nb = 0; nb < NBW; ++nb) wa[nb] = XB + (8 * g + q) * WRB + ((wu[nb] ^ wswz<WRB>(8 * g + q)) << 5) + p * 8;
    bf16x8 ones;
#pragma unroll
    for (int j = 0; j < 8; ++j) ones[j] = 0x3F80;
    int sb = 0;
    auto ld = [&](Frags& f) {
#pragma unroll
      for (int ks = 0; ks < 2; ++ks) {
#pragma unroll
        for (int nb = 0; nb < NBW; ++nb) {
          s16x4 lo = trread(L + sb + wa[nb] + (32 * ks) * WRB), hi = trread(L + sb + wa[nb] + (32 * ks + 4) * WRB);
          f.w[ks][nb] = __builtin_shufflevector(lo, hi, 0, 1, 2, 3, 4, 5, 6, 7);
        }
#pragma unroll
        for (int mb = 0; mb < MBW; ++mb)
          f.x[ks][mb] = *(const __attribute__((address_space(3))) bf16x8*)(L + sb + ((xa0 ^ (ks * 64)) + mb * 2048));
      }
      sb += STG; if (sb == RING) sb = 0;
    };
    auto mm = [&](const Frags& f) {
#pragma unroll
      for (int ks = 0; ks < 2; ++ks)
#pragma unroll
        for (int nb = 0; nb < NBW; ++nb) {
#pragma unroll
          for (int mb = 0; mb < MBW; ++mb) {
            if (VT && nb == NBW - 1) acc[nb][mb] = MFMA16(f.x[ks][mb], f.w[ks][nb], acc[nb][mb]);
            else acc[nb][mb] = MFMA16(f.w[ks][nb], f.x[ks][mb], acc[nb][mb]);
          }
          if constexpr (LN != 0) {
            if (VT && nb == NBW - 1) cs[nb] = MFMA16(ones, f.w[ks][nb], cs[nb]);
            else cs[nb] = MFMA16(f.w[ks][nb], ones, cs[nb]);
          }
        }
    };
    constexpr int NM = 2 * NBW * (MBW + (LN != 0 ? 1 : 0)), NRD = 2 * (2 * NBW + MBW), RPM = (NRD + NM - 1) / NM;
    auto step = [&](Frags& nx, const Frags& cu) {
      SB0(); ld(nx); mm(cu);
#pragma unroll
      for (int i = 0; i < NM; ++i) { __builtin_amdgcn_sched_group_barrier(0x008, 1, 0); __builtin_amdgcn_sched_group_barrier(0x100, RPM, 0); }
      SB0();
    };
    Frags A, B;
    const char* landed = lds + FLG;
    const unsigned done = (unsigned)(uintptr_t)(lds + FLG + 16) + wid * 4;
    unsigned fl = 0;
    auto sync = [&](int sdone, int need) {
      asm volatile("s_waitcnt lgkmcnt(0)" ::: "memory");
      flag_store(done, (unsigned)sdone);
      if (fl < (unsigned)need) { fl = min4(landed); while (fl < (unsigned)need) { __builtin_amdgcn_s_sleep(1); fl = min4(landed); } }
    };
    sync(0, 1); ld(A);
    if (wid == 0) STAMP(kid, 1);
#pragma unroll 1
    for (int s = 0; s + 2 < NK; s += 2) {
      sync(s + 1, s + 2); step(B, A);
      sync(s + 2, s + 3); step(A, B);
    }
    if constexpr (NK % 2 == 0) { sync(NK - 1, NK); step(B, A); mm(B); }
    else { mm(A); }
    SB0();
    if constexpr (LN != 0) bar();
  }
};

DEV void tile_of(int& rt, int& ct) { int b = blockIdx.x, xcd = b & 7, idx = b >> 3; rt = xcd * 4 + (idx & 3); ct = idx >> 2; }
DEV void warm_share(const void* p, int bytes, int w4, int lane, unsigned junk) { const int sh = bytes / 128; l2_touch((const char*)p + (size_t)((blockIdx.x >> 3) * 4 + w4) * sh, sh, lane, junk); }

DEV s16x4 pack4(f32x4 v) { u32x2 p = {cvtpk(v[0], v[1]), cvtpk(v[2], v[3])}; return __builtin_bit_cast(s16x4, p); }
#define MFMA16K16(a, b, c) __builtin_amdgcn_mfma_f32_16x16x16bf16_1k(a, b, c, 0, 0, 0)
template <int LNM>
DEV void k1_body(const u16* __restrict__ x, const float* __restrict__ part, const u16* __restrict__ wqkv, u16* __restrict__ o, u64* zero_buf, const u16* __restrict__ wnext, const u16* w2, const u16* w3, const u16* w4, int kid) {
  if (threadIdx.x == 0) STAMP(kid, 0);
  using C = Core<80, 384, 8, 4, 1, 5, 3, LNM, 0, true>;
  __shared__ __attribute__((aligned(16))) char lds[C::LDS_BYTES];
  const int tid = threadIdx.x, lane = tid & 63, wid = __builtin_amdgcn_readfirstlane(tid >> 6);
  int rt, ct; tile_of(rt, ct); const int r0 = rt * 64;
  C::flags_init(lds);
  if (zero_buf && tid < 128) zero_buf[blockIdx.x * 128 + tid] = 0ull;
  if (wid >= 4) {
    C c; c.init((unsigned)(uintptr_t)lds, wid - 4, lane, x, DM, wqkv, 3 * DM,
              [&](int r) { int gr = r0 - 16 + r; return gr < 0 ? 0 : gr; },
              [&](int u) { return (u >> 2) * DM + (ct * 4 + (u & 3)) * 16; });
    c.load_loop(lds, wid - 4, kid);
    if constexpr (LNM == 2) {
      const unsigned junk = (unsigned)(uintptr_t)lds + C::JUNK; const int part_ = blockIdx.x * 4 + wid - 4;
      touch_always((const char*)wnext + (size_t)part_ * 512, 512, lane, junk);
      if (part_ < 688) touch_always((const char*)w2 + (size_t)part_ * 2048, 2048, lane, junk);
      touch_always((const char*)w3 + (size_t)part_ * 8192, 8192, lane, junk);
      if (part_ < 688) touch_always((const char*)w4 + (size_t)part_ * 1024, 1024, lane, junk);
    }
    if (wid == 4) STAMP(kid, 2);
    return;
  }
  if constexpr (LNM == 1) C::stats_from(lds, tid, part, [&](int r) { int gr = r0 - 16 + r; return gr < 0 ? 0 : gr; });
  if constexpr (LNM == 2) {
    const unsigned junk = (unsigned)(uintptr_t)lds + C::JUNK;
    const int off = (((blockIdx.x >> 3) * 4 + wid) * 64 + lane) * 128;
    auto warm = [&](unsigned lo, unsigned hi, int bytes) {
      if (off < bytes) {
        const char* p = (const char*)(((u64)hi << 32) | lo) + off; unsigned sv;
        asm volatile("s_mov_b32 %0, m0\n\ts_mov_b32 m0, %2\n\ts_nop 0\n\tglobal_load_lds_dword %1, off\n\ts_mov_b32 m0, %0" : "=&s"(sv) : "v"(p), "s"(junk) : "memory");
      }
    };
#define CODE_ADDR(SYM, BYTES) do { unsigned lo_, hi_; asm volatile("s_getpc_b64 s[40:41]\n\ts_add_u32 s40, s40, " #SYM "@rel32@lo+4\n\ts_addc_u32 s41, s41, " #SYM "@rel32@hi+12\n\ts_mov_b32 %0, s40\n\ts_mov_b32 %1, s41" : "=s"(lo_), "=s"(hi_) :: "s40", "s41", "scc"); warm(lo_, hi_, BYTES); } while (0)
    CODE_ADDR(k2_wo, CW_K2); CODE_ADDR(k3_win, CW_K3); CODE_ADDR(k4_wsp, CW_K4); CODE_ADDR(k5_wout, CW_K5); CODE_ADDR(k1_next, CW_K1);
  }
  f32x4 acc[3][5] = {}, cs[3] = {};
  const int wu[3] = {wid, 4 + wid, 8 + wid};
  C::run(lds, wid, lane, 0, wu, acc, cs, kid); if (wid == 0) STAMP(kid, 3);
  const float* stt = (const float*)(lds + C::RING);
  const int g = lane >> 4, m16 = lane & 15;
  s16x4 qf[5], kf[5], vf[5];
#pragma unroll
  for (int mb = 0; mb < 5; ++mb) {
    const float rs = stt[(mb * 16 + m16) * 2 + 1], cm = -rs * stt[(mb * 16 + m16) * 2];
    if (mb > 0) qf[mb] = pack4(rs * acc[0][mb] + cm * cs[0]);
    kf[mb] = pack4(rs * acc[1][mb] + cm * cs[1]);
    const f32x4 sa = *(const f32x4*)(stt + (mb * 16 + 4 * g) * 2), sb2 = *(const f32x4*)(stt + (mb * 16 + 4 * g) * 2 + 4);
    f32x4 v = {sa[1] * (acc[2][mb][0] - sa[0] * cs[2][0]), sa[3] * (acc[2][mb][1] - sa[2] * cs[2][0]),
               sb2[1] * (acc[2][mb][2] - sb2[0] * cs[2][0]), sb2[3] * (acc[2][mb][3] - sb2[2] * cs[2][0])};
    vf[mb] = pack4(v);
  }
  bool v0[4], v1[4];
#pragma unroll
  for (int j = 0; j < 4; ++j) { int d0 = m16 + 16 - (4 * g + j), d1 = m16 - (4 * g + j); v0[j] = d0 <= 14 && !(d0 & 1); v1[j] = d1 >= 0 && d1 <= 14 && !(d1 & 1); }
  const float sc = 0.25f * 1.4426950408889634f;
#pragma unroll
  for (int qb = 0; qb < 4; ++qb) {
    SB0();
    f32x4 s0 = MFMA16K16(kf[qb], qf[qb + 1], (f32x4{})), s1 = MFMA16K16(kf[qb + 1], qf[qb + 1], (f32x4{}));
    const bool prev_ok = (r0 + qb * 16) > 0;
    float mx = -3.0e38f;
#pragma unroll
    for (int j = 0; j < 4; ++j) {
      s0[j] = (v0[j] && prev_ok) ? s0[j] * sc : -1.0e9f; s1[j] = v1[j] ? s1[j] * sc : -1.0e9f;
      mx = fmaxf(mx, fmaxf(s0[j], s1[j]));
    }
    mx = max_g(mx);
    float sum = 0.f;
#pragma unroll
    for (int j = 0; j < 4; ++j) { s0[j] = __builtin_amdgcn_exp2f(s0[j] - mx); s1[j] = __builtin_amdgcn_exp2f(s1[j] - mx); sum += s0[j] + s1[j]; }
    sum = sum_g(sum);
    const float inv = __builtin_amdgcn_rcpf(sum);
    f32x4 ot = MFMA16K16(vf[qb], pack4(s0 * inv), (f32x4{}));
    ot = MFMA16K16(vf[qb + 1], pack4(s1 * inv), ot);
    *(u32x2*)(o + (size_t)(r0 + qb * 16 + m16) * DM + (ct * 4 + wid) * 16 + 4 * g) = u32x2{cvtpk(ot[0], ot[1]), cvtpk(ot[2], ot[3])};
  }
  if (wid == 0) STAMP(kid, 4);
}

template <int NK, int LDA, int KCLAMP>
DEV void proj_body(const u16* __restrict__ a, const u16* __restrict__ w, const u16* __restrict__ res, u16* __restrict__ out, float* __restrict__ part, const u16* __restrict__ wnext, int wnext_bytes, int kid) {
  if (threadIdx.x == 0) STAMP(kid, 0);
  using C = Core<64, 128, NK, 8, 2, 2, 2, 0, KCLAMP>;
  __shared__ __attribute__((aligned(16))) char lds[C::LDS_BYTES];
  const int tid = threadIdx.x, lane = tid & 63, wid = __builtin_amdgcn_readfirstlane(tid >> 6);
  int rt, ct; tile_of(rt, ct); const int r0 = rt * 64, c0 = ct * 64;
  C::flags_init(lds);
  if (wid >= 4) {
    C c; c.init((unsigned)(uintptr_t)lds, wid - 4, lane, a, LDA, w, DM, [&](int r) { return r0 + r; }, [&](int u) { return c0 + u * 16; });
    c.load_loop(lds, wid - 4, kid);
    if (wnext) warm_share(wnext, wnext_bytes, wid - 4, lane, (unsigned)(uintptr_t)lds + C::JUNK); if (wid == 4) STAMP(kid, 2); return;
  }
  const int wm = wid >> 1, wn = wid & 1, g = lane >> 4, m16 = lane & 15;
  u32x2 rv[2][2];
#pragma unroll
  for (int nb = 0; nb < 2; ++nb)
#pragma unroll
    for (int mb = 0; mb < 2; ++mb) rv[nb][mb] = *(const u32x2*)(res + (size_t)(r0 + wm * 32 + mb * 16 + m16) * DM + c0 + (wn * 2 + nb) * 16 + g * 4);
  f32x4 acc[2][2] = {}, cs[2];
  const int wu[2] = {wn * 2, wn * 2 + 1};
  C::run(lds, wid, lane, wm * 32, wu, acc, cs, kid); if (wid == 0) STAMP(kid, 3);
#pragma unroll
  for (int mb = 0; mb < 2; ++mb) {
    float ps = 0.f, pq = 0.f;
#pragma unroll
    for (int nb = 0; nb < 2; ++nb) {
      f32x4 A = acc[nb][mb]; u32x2 r = rv[nb][mb];
      float v0 = bf2f(r[0] & 0xFFFF) + rbf(A[0]), v1 = bf2f(r[0] >> 16) + rbf(A[1]), v2 = bf2f(r[1] & 0xFFFF) + rbf(A[2]), v3 = bf2f(r[1] >> 16) + rbf(A[3]);
      u32x2 o = {cvtpk(v0, v1), cvtpk(v2, v3)};
      *(u32x2*)(out + (size_t)(r0 + wm * 32 + mb * 16 + m16) * DM + c0 + (wn * 2 + nb) * 16 + g * 4) = o;
      v0 = bf2f(o[0] & 0xFFFF); v1 = bf2f(o[0] >> 16); v2 = bf2f(o[1] & 0xFFFF); v3 = bf2f(o[1] >> 16);
      ps += (v0 + v1) + (v2 + v3); pq += (v0 * v0 + v1 * v1) + (v2 * v2 + v3 * v3);
    }
    ps = sum_g(ps); pq = sum_g(pq);
    if (g == 0) *(float2*)(part + (size_t)(r0 + wm * 32 + mb * 16 + m16) * 32 + (ct * 2 + wn) * 2) = make_float2(ps, pq);
  }
  if (wid == 0) STAMP(kid, 4);
}

typedef __attribute__((ext_vector_type(2))) float f32x2;
DEV f32x2 gelu2(f32x2 t) {
  const float c1 = -1.5957691216057308f * 1.4426950408889634f, c2 = c1 * 0.044715f;
  f32x2 y = t * (t * t * c2 + c1);
  f32x2 d = {__builtin_amdgcn_exp2f(y[0]), __builtin_amdgcn_exp2f(y[1])};
  d += 1.f;
  f32x2 r = {__builtin_amdgcn_rcpf(d[0]), __builtin_amdgcn_rcpf(d[1])};
  return t * r;
}
extern "C" __global__ __launch_bounds__(512, 2) void k3_win(const u16* __restrict__ x, const float* __restrict__ part, const u16* __restrict__ w_in, u16* __restrict__ z1, u16* __restrict__ z2n, u64* xch, unsigned tag, const u16* __restrict__ w_sp, int kid) {
  if (threadIdx.x == 0) STAMP(kid, 0);
  using C = Core<64, 384, 8, 4, 1, 4, 3, 1, 0>;
  __shared__ __attribute__((aligned(16))) char lds[C::LDS_BYTES];
  const int tid = threadIdx.x, lane = tid & 63, wid = __builtin_amdgcn_readfirstlane(tid >> 6), w4 = wid & 3;
  int rt, ct; tile_of(rt, ct); const int r0 = rt * 64;
  C::flags_init(lds);
  const int half = ct >> 2, zt = ct & 3, nun = zt == 3 ? 10 : 11, f0 = zt * 176;
  f32x4 a2[3][2], cs[3];
  f32x4* ho = (f32x4*)(lds + 4096) + w4 * (9 * 64) + lane;
  if (wid >= 4) {
    C c; c.init((unsigned)(uintptr_t)lds, wid - 4, lane, x, DM, w_in, FF, [&](int r) { return r0 + r; },
              [&](int u) { return half * FH + f0 + (u < nun ? u : 0) * 16; });
    c.load_loop(lds, wid - 4, kid);
    if (wid == 4) STAMP(kid, 2);
    bar();
#pragma unroll
    for (int nb = 0; nb < 3; ++nb) { a2[nb][0] = ho[(nb * 3) * 64]; a2[nb][1] = ho[(nb * 3 + 1) * 64]; cs[nb] = ho[(nb * 3 + 2) * 64]; }
  } else {
    C::stats_from(lds, tid, part, [&](int r) { return r0 + r; });
    f32x4 acc[3][4] = {};
#pragma unroll
    for (int nb = 0; nb < 3; ++nb) cs[nb] = f32x4{};
    const int wu[3] = {wid * 3, wid * 3 + 1, wid * 3 + 2};
    C::run(lds, wid, lane, 0, wu, acc, cs, kid); if (wid == 0) STAMP(kid, 3);
#pragma unroll
    for (int nb = 0; nb < 3; ++nb) { ho[(nb * 3) * 64] = acc[nb][2]; ho[(nb * 3 + 1) * 64] = acc[nb][3]; ho[(nb * 3 + 2) * 64] = cs[nb]; a2[nb][0] = acc[nb][0]; a2[nb][1] = acc[nb][1]; }
    bar();
  }
  const float* stt = (const float*)(lds + C::RING);
  const int g = lane >> 4, m16 = lane & 15, rb = (wid >> 2) * 32;
  u32x2 pk[3][2]; float p1[2], p2[2];
#pragma unroll
  for (int mb = 0; mb < 2; ++mb) {
    const float rs = stt[(rb + mb * 16 + m16) * 2 + 1], cm = -rs * stt[(rb + mb * 16 + m16) * 2];
    f32x2 s1 = {0.f, 0.f}, s2 = {0.f, 0.f};
#pragma unroll
    for (int nb = 0; nb < 3; ++nb) {
      f32x4 t = rs * a2[nb][mb] + cm * cs[nb];
      f32x2 lo = gelu2(f32x2{t[0], t[1]}), hi = gelu2(f32x2{t[2], t[3]});
      pk[nb][mb] = u32x2{cvtpk(lo[0], lo[1]), cvtpk(hi[0], hi[1])};
      if (w4 * 3 + nb < nun) {
        f32x2 a = {bf2f(pk[nb][mb][0] & 0xFFFF), bf2f(pk[nb][mb][0] >> 16)}, b = {bf2f(pk[nb][mb][1] & 0xFFFF), bf2f(pk[nb][mb][1] >> 16)};
        s1 += a + b; s2 += a * a + b * b;
      }
    }
    p1[mb] = s1[0] + s1[1]; p2[mb] = s2[0] + s2[1];
  }
  if (half == 0) {
#pragma unroll
    for (int mb = 0; mb < 2; ++mb)
#pragma unroll
      for (int nb = 0; nb < 3; ++nb)
        if (w4 * 3 + nb < nun) *(u32x2*)(z1 + (size_t)(r0 + rb + mb * 16 + m16) * FH + f0 + (w4 * 3 + nb) * 16 + g * 4) = pk[nb][mb];
    if (wid == 0) STAMP(kid, 4);
    return;
  }
  if (wid == 0) STAMP(kid, 5);
  float* red = (float*)lds;
  float* tot = red + 512;
#pragma unroll
  for (int mb = 0; mb < 2; ++mb) {
    float a = p1[mb], b = p2[mb];
    a = sum_g(a); b = sum_g(b);
    if (g == 0) *(float2*)(red + (w4 * 64 + rb + mb * 16 + m16) * 2) = make_float2(a, b);
  }
  __syncthreads();
  if (tid < 128) {
    float own = (red[tid] + red[128 + tid]) + (red[256 + tid] + red[384 + tid]);
    u64* base = xch + (size_t)rt * 4 * 128;
    __hip_atomic_store(base + zt * 128 + tid, ((u64)tag << 32) | __float_as_uint(own), __ATOMIC_RELAXED, __HIP_MEMORY_SCOPE_AGENT);
    u64 v[4]; int spins = 0; bool ok;
    do {
#pragma unroll
      for (int t = 0; t < 4; ++t) v[t] = __hip_atomic_load(base + t * 128 + tid, __ATOMIC_RELAXED, __HIP_MEMORY_SCOPE_AGENT);
      ok = true;
#pragma unroll
      for (int t = 0; t < 4; ++t) ok = ok && (t == zt || (unsigned)(v[t] >> 32) == tag);
    } while (!ok && ++spins < (1 << 22));
    float pt[4];
#pragma unroll
    for (int t = 0; t < 4; ++t) pt[t] = t == zt ? own : __uint_as_float((unsigned)v[t]);
    tot[tid] = (pt[0] + pt[1]) + (pt[2] + pt[3]);
  }
  __syncthreads();
  if (wid == 0) STAMP(kid, 6);
#pragma unroll
  for (int mb = 0; mb < 2; ++mb) {
    const int r = rb + mb * 16 + m16;
    const float mean = tot[r * 2] * (1.f / FH), var = fmaxf(tot[r * 2 + 1] * (1.f / FH) - mean * mean, 0.f);
    const float rs = rsqrtf(var + 1e-5f), cm = -mean * rs;
#pragma unroll
    for (int nb = 0; nb < 3; ++nb) {
      const int f = f0 + (w4 * 3 + nb) * 16;
      if (w4 * 3 + nb < nun) {
        u32x2 v = pk[nb][mb];
        *(u32x2*)(z2n + ((size_t)(f / 96) * SEQ + r0 + r) * 96 + f % 96 + g * 4) =
            u32x2{cvtpk(fmaf(bf2f(v[0] & 0xFFFF), rs, cm), fmaf(bf2f(v[0] >> 16), rs, cm)), cvtpk(fmaf(bf2f(v[1] & 0xFFFF), rs, cm), fmaf(bf2f(v[1] >> 16), rs, cm))};
      }
    }
  }
  if (wid == 0) STAMP(kid, 4);
}

extern "C" __global__ __launch_bounds__(512, 2) void k4_wsp(const u16* __restrict__ w_sp, const u16* __restrict__ z2n, const u16* __restrict__ z1, const u16* __restrict__ b_sp, u16* __restrict__ u, const u16* __restrict__ wnext, int kid) {
  if (threadIdx.x == 0) STAMP(kid, 0);
  using C = Core<64, 192, 32, 7, 2, 2, 3, 0, 0>;
  __shared__ __attribute__((aligned(16))) char lds[C::LDS_BYTES];
  const int tid = threadIdx.x, lane = tid & 63, wid = __builtin_amdgcn_readfirstlane(tid >> 6);
  int rt, ct; tile_of(rt, ct); const int r0 = rt * 64, c0 = ct * 96;
  C::flags_init(lds);
  if (wid >= 4) {
    C c; c.init((unsigned)(uintptr_t)lds, wid - 4, lane, w_sp, SEQ, z2n + (size_t)ct * SEQ * 96, 96, [&](int r) { return r0 + r; },
              [&](int uu) { return uu * 16; });
    c.load_loop(lds, wid - 4, kid);
    warm_share(wnext, FH * DM * 2, wid - 4, lane, (unsigned)(uintptr_t)lds + C::JUNK); if (wid == 4) STAMP(kid, 2); return;
  }
  const int wm = wid >> 1, wn = wid & 1, g = lane >> 4, m16 = lane & 15;
  u32x2 zv[3][2]; float bv[2];
#pragma unroll
  for (int mb = 0; mb < 2; ++mb) {
    bv[mb] = bf2f(b_sp[r0 + wm * 32 + mb * 16 + m16]);
#pragma unroll
    for (int nb = 0; nb < 3; ++nb) {
      int col = c0 + (wn * 3 + nb) * 16; if (col >= FH) col = 0;
      zv[nb][mb] = *(const u32x2*)(z1 + (size_t)(r0 + wm * 32 + mb * 16 + m16) * FH + col + g * 4);
    }
  }
  f32x4 acc[3][2] = {}, cs[3];
  const int wu[3] = {wn * 3, wn * 3 + 1, wn * 3 + 2};
  C::run(lds, wid, lane, wm * 32, wu, acc, cs, kid); if (wid == 0) STAMP(kid, 3);
#pragma unroll
  for (int nb = 0; nb < 3; ++nb) {
    const int col = c0 + (wn * 3 + nb) * 16;
#pragma unroll
    for (int mb = 0; mb < 2; ++mb) {
      f32x4 A = acc[nb][mb]; u32x2 z = zv[nb][mb]; float b = bv[mb];
      float v0 = bf2f(z[0] & 0xFFFF) * rbf(rbf(A[0]) + b), v1 = bf2f(z[0] >> 16) * rbf(rbf(A[1]) + b);
      float v2 = bf2f(z[1] & 0xFFFF) * rbf(rbf(A[2]) + b), v3 = bf2f(z[1] >> 16) * rbf(rbf(A[3]) + b);
      u32x2 r = {cvtpk(v0, v1), cvtpk(v2, v3)};
      if (col >= FH) r = u32x2{0u, 0u};
      if (col < UP) *(u32x2*)(u + (size_t)(r0 + wm * 32 + mb * 16 + m16) * UP + col + g * 4) = r;
    }
  }
  if (wid == 0) STAMP(kid, 4);
}

#define K1_ARGS const u16* x, const float* part, const u16* wqkv, u16* o, u64* zero_buf, const u16* wnext, const u16* w2, const u16* w3, const u16* w4, int kid
extern "C" __global__ __launch_bounds__(512, 2) void k1_first(K1_ARGS) { k1_body<2>(x, part, wqkv, o, zero_buf, wnext, w2, w3, w4, kid); }
extern "C" __global__ __launch_bounds__(512, 2) void k1_next(K1_ARGS) { k1_body<1>(x, part, wqkv, o, zero_buf, wnext, w2, w3, w4, kid); }
#define PJ_ARGS const u16* a, const u16* w, const u16* res, u16* out, float* part, const u16* wnext, int wnext_bytes, int kid
extern "C" __global__ __launch_bounds__(512, 2) void k2_wo(PJ_ARGS) { proj_body<8, DM, 0>(a, w, res, out, part, wnext, wnext_bytes, kid); }
extern "C" __global__ __launch_bounds__(512, 2) void k5_wout(PJ_ARGS) { proj_body<11, UP, FH>(a, w, res, out, part, wnext, wnext_bytes, kid); }

extern "C" void kernel_launch(void* const* d_in, const int* in_sizes, int n_in, void* d_out, int out_size, void* d_ws, size_t ws_size, hipStream_t stream) {
  const u16* x = (const u16*)d_in[0]; const u16* wqkv = (const u16*)d_in[1]; const u16* wo = (const u16*)d_in[2];
  const u16* w_in = (const u16*)d_in[4]; const u16* w_sp = (const u16*)d_in[5]; const u16* b_sp = (const u16*)d_in[6]; const u16* w_out = (const u16*)d_in[7];
  char* ws = (char*)d_ws;
  u64* xch = (u64*)ws;
  u16* o = (u16*)(ws + (1 << 20)); u16* x1 = o + SEQ * DM; u16* x2 = x1 + SEQ * DM;
  u16* z1 = x2 + SEQ * DM; u16* z2n = z1 + SEQ * FH; u16* u = z2n + SEQ * 768;
  float* part = (float*)(ws + (512 << 10));
  const u16* xin = x;
  for (int L = 0; L < NLAYERS; ++L) {
    u16* xo = L == 0 ? x2 : (u16*)d_out;
    if (L == 0) k1_first<<<256, 512, 0, stream>>>(xin, part, wqkv, o, xch, wo, w_in, w_sp, w_out, L * 5 + 0);
    else k1_next<<<256, 512, 0, stream>>>(xin, part, wqkv, o, nullptr, wo, w_in, w_sp, w_out, L * 5 + 0);
    k2_wo<<<256, 512, 0, stream>>>(o, wo, xin, x1, part, w_in, DM * FF * 2, L * 5 + 1);
    k3_win<<<256, 512, 0, stream>>>(x1, part, w_in, z1, z2n, xch + (size_t)L * 32 * 4 * 128, 0x5A5A0001u + L, w_sp, L * 5 + 2);
    k4_wsp<<<256, 512, 0, stream>>>(w_sp, z2n, z1, b_sp, u, w_out, L * 5 + 3);
    k5_wout<<<256, 512, 0, stream>>>(u, w_out, x1, xo, part, L == 0 ? wqkv : nullptr, DM * 3 * DM * 2, L * 5 + 4);
    xin = xo;
  }
}
```

```cpp
#define EARLY_WARM
#define CODE_WARM
#include <hip/hip_runtime.h>
#include <hip/hip_bf16.h>
#include <stdint.h>
#include <type_traits>

#define NLAYERS 2
typedef unsigned short u16;
typedef unsigned long long u64;
#define CW_K1 9088
#define CW_K2 3328
#define CW_K3 8832
#define CW_K4 4608
#define CW_K5 3200
typedef __attribute__((ext_vector_type(8))) short bf16x8;
typedef __attribute__((ext_vector_type(4))) short s16x4;
typedef __attribute__((ext_vector_type(4))) float f32x4;
typedef __attribute__((ext_vector_type(2))) __bf16 bf16x2;
typedef __attribute__((ext_vector_type(4))) unsigned u32x4;
typedef __attribute__((ext_vector_type(2))) unsigned u32x2;
typedef __attribute__((address_space(3))) const char* lds_cptr;
typedef short v4i16_t __attribute__((ext_vector_type(4)));
#define DEV __device__ __forceinline__

constexpr int SEQ = 2048, DM = 512, FH = 688, FF = 1376, UP = 704;

template <int I, int N, class F> DEV void static_for(F&& f) {
  if constexpr (I < N) { f(std::integral_constant<int, I>{}); static_for<I + 1, N>(f); }
}
DEV void glds16(const void* g, unsigned lds_base) {
  unsigned sv;
  asm volatile("s_mov_b32 %0, m0\n\ts_mov_b32 m0, %2\n\ts_nop 0\n\tglobal_load_lds_dwordx4 %1, off\n\ts_mov_b32 m0, %0"
               : "=&s"(sv) : "v"(g), "s"(lds_base) : "memory");
}
template <int IMM> DEV void glds16s(unsigned voff, const void* sbase, unsigned ldsbase) {
  asm volatile("s_add_u32 m0, %1, %2\n\ts_nop 0\n\tglobal_load_lds_dwordx4 %0, %3" ::"v"(voff), "s"(ldsbase), "i"(IMM), "s"(sbase) : "memory", "scc");
}
template <int N> DEV void wait_bar() { asm volatile("s_waitcnt vmcnt(%0) lgkmcnt(0)\n\ts_barrier" ::"i"(N) : "memory"); }
DEV s16x4 trread(lds_cptr p) {
  return __builtin_bit_cast(s16x4, __builtin_amdgcn_ds_read_tr16_b64_v4i16((__attribute__((address_space(3))) v4i16_t*)p));
}
DEV float bf2f(unsigned v) { return __uint_as_float(v << 16); }
DEV unsigned cvtpk(float lo, float hi) { bf16x2 v = {(__bf16)lo, (__bf16)hi}; return __builtin_bit_cast(unsigned, v); }
DEV float rbf(float f) { return __uint_as_float(cvtpk(f, f) << 16); }
DEV float dot2(unsigned a, unsigned b, float c) {
  return __builtin_amdgcn_fdot2_f32_bf16(__builtin_bit_cast(bf16x2, a), __builtin_bit_cast(bf16x2, b), c, false);
}
template <int CTRL> DEV float dpp(float v) { return __int_as_float(__builtin_amdgcn_mov_dpp(__float_as_int(v), CTRL, 0xF, 0xF, true)); }
DEV float sum_x1(float v) { return v + dpp<0xB1>(v); }
DEV float sum_x2(float v) { return v + dpp<0x4E>(v); }
DEV float sum_x4(float v) { return v + dpp<0x141>(v); }
DEV float sum_g(float v) {
  auto a = __builtin_amdgcn_permlane16_swap(__float_as_uint(v), __float_as_uint(v), false, false);
  v = __uint_as_float(a[0]) + __uint_as_float(a[1]);
  auto b = __builtin_amdgcn_permlane32_swap(__float_as_uint(v), __float_as_uint(v), false, false);
  return __uint_as_float(b[0]) + __uint_as_float(b[1]);
}
DEV float max_g(float v) {
  auto a = __builtin_amdgcn_permlane16_swap(__float_as_uint(v), __float_as_uint(v), false, false);
  v = fmaxf(__uint_as_float(a[0]), __uint_as_float(a[1]));
  auto b = __builtin_amdgcn_permlane32_swap(__float_as_uint(v), __float_as_uint(v), false, false);
  return fmaxf(__uint_as_float(b[0]), __uint_as_float(b[1]));
}
#define SB0() __builtin_amdgcn_sched_barrier(0)
#define MFMA16(a, b, c) __builtin_amdgcn_mfma_f32_16x16x32_bf16(a, b, c, 0, 0, 0)

#define STAMP(KID, IDX) do { } while (0)
DEV void l2_touch(const void* p, int bytes, int lane, unsigned junk_lds) {
  return;
  const char* a = (const char*)p + lane * 128;
#pragma unroll 1
  for (int off = lane * 128; off < bytes; off += 8192, a += 8192) {
    unsigned sv;
    asm volatile("s_mov_b32 %0, m0\n\ts_mov_b32 m0, %2\n\ts_nop 0\n\tglobal_load_lds_dword %1, off\n\ts_mov_b32 m0, %0"
                 : "=&s"(sv) : "v"(a), "s"(junk_lds) : "memory");
  }
}
DEV void touch_always(const void* p, int bytes, int lane, unsigned junk_lds) {
  const char* a = (const char*)p + lane * 128;
#pragma unroll 1
  for (int off = lane * 128; off < bytes; off += 8192, a += 8192) {
    unsigned sv;
    asm volatile("s_mov_b32 %0, m0\n\ts_mov_b32 m0, %2\n\ts_nop 0\n\tglobal_load_lds_dword %1, off\n\ts_mov_b32 m0, %0"
                 : "=&s"(sv) : "v"(a), "s"(junk_lds) : "memory");
  }
}
template <int WRB> DEV int wswz(int k) {
  if constexpr (WRB % 256 == 128) return ((k >> 1) & 1) | (((k >> 3) & 1) << 1);
  else return (k >> 3) & 1;
}

template <int N> DEV void wait_vm() { asm volatile("s_waitcnt vmcnt(%0)" ::"i"(N) : "memory"); }
DEV void bar() { asm volatile("s_waitcnt lgkmcnt(0)\n\ts_barrier" ::: "memory"); }

template <int XR, int WRB, int NK, int S, int R, int MBW, int NBW, int LN, int KCLAMP, bool VT = false>
struct Core {
  static constexpr int XI = (XR * 2 + 63) / 64, WI = WRB / 64, LPS = XI + WI;
  static constexpr int XB = XR * 128, STG = XB + 64 * WRB, RING = S * STG, NR = (NK + R - 1) / R;
  static constexpr int JUNK = RING + XR * 8, FLG = JUNK + 256, LDS_BYTES = FLG + 32;
  static constexpr int D = 2;
  unsigned xo[XI], wo_[WI]; int wk[WI];
  const char* xb; const char* wb;
  unsigned ldsx, ldsw; int ldw; int lane;

  template <class FR, class FC>
  DEV void init(unsigned lds0, int wid, int lane_, const u16* X, int ldx, const u16* W, int ldw_, FR xrow, FC wcol) {
    lane = lane_; ldw = ldw_; xb = (const char*)X; wb = (const char*)W;
    ldsx = lds0 + wid * (XR * 32); ldsw = lds0 + XB + wid * (16 * WRB);
#pragma unroll
    for (int i = 0; i < XI; ++i) {
      int ci = i * 64 + lane; if (ci >= XR * 2) ci = XR * 2 - 1;
      int r = wid * (XR / 4) + (ci >> 3), pos = ci & 7;
      xo[i] = (unsigned)(xrow(r) * ldx + ((pos ^ (r & 7)) << 3)) * 2u;
    }
#pragma unroll
    for (int i = 0; i < WI; ++i) {
      int b = i * 1024 + lane * 16; int kr = wid * 16 + b / WRB, ir = b % WRB;
      int u = (ir >> 5) ^ wswz<WRB>(kr);
      wk[i] = kr;
      wo_[i] = (unsigned)(kr * ldw + wcol(u) + ((ir >> 4) & 1) * 8) * 2u;
    }
  }
  template <class P> DEV void issue(int st, unsigned sb, P&& poll) {
    const unsigned lx = ldsx + sb, lw = ldsw + sb;
    static_for<0, XI>([&](auto ic) {
      constexpr int i = ic.value;
      if constexpr ((XR * 2) % 64 != 0 && i == XI - 1) { if (lane < (XR * 2) % 64) glds16s<i * 1024>(xo[i], xb, lx); }
      else glds16s<i * 1024>(xo[i], xb, lx);
    });
    poll(st * LPS + XI - 1);
    if constexpr (KCLAMP > 0) {
      if (st == NK - 1) {
#pragma unroll
        for (int i = 0; i < WI; ++i) { int over = st * 64 + wk[i] - (KCLAMP - 1); if (over > 0) wo_[i] -= (unsigned)(over * ldw) * 2u; }
      }
    }
    static_for<0, WI>([&](auto ic) {
      constexpr int i = ic.value;
      glds16s<i * 1024>(wo_[i], wb, lw);
      if constexpr (i % 3 == 2 || i == WI - 1) poll(st * LPS + XI + i);
    });
    xb += 128; wb += (size_t)128 * ldw;
  }
  static DEV void wait_stages(int n) {
    static_for<0, S>([&](auto rc) { if (n == rc.value) wait_vm<rc.value * LPS>(); });
  }
  static DEV void flags_init(char*) {}
  DEV void load_loop(char* lds, int wid, int kid) {
    static_assert(S <= NK && R < S, "ring shape");
    unsigned sb = 0; int issued = 0;
    auto push = [&](int lim) {
#pragma unroll 1
      while (issued < lim) { issue(issued, sb, [](int) {}); ++issued; sb += STG; if (sb == RING) sb = 0; }
    };
    f32x4 s1[XI] = {}, s2[XI] = {};
    auto stats = [&](int st) {
      if constexpr (LN == 2) {
        const unsigned cb = (unsigned)(st % S) * STG;
#pragma unroll
        for (int i = 0; i < XI; ++i) {
          u32x4 v = *(const u32x4*)(lds + cb + wid * (XR * 32) + i * 1024 + lane * 16);
#pragma unroll
          for (int j = 0; j < 4; ++j) {
            float lo = __uint_as_float(v[j] << 16), hi = __uint_as_float(v[j] & 0xFFFF0000u);
            s1[i][j] += lo + hi; s2[i][j] = fmaf(lo, lo, fmaf(hi, hi, s2[i][j]));
          }
        }
      }
    };
    push(R + 1);
    wait_stages(R); bar();
    if (wid == 0) STAMP(kid, 1);
    stats(0);
#pragma unroll 1
    for (int j = 0; j < NR; ++j) {
      int lim = (j == 0) ? S : (j - 1) * R + 1 + S; if (lim > NK) lim = NK;
      if (lim > issued + 2 * R) lim = issued + 2 * R;
      push(lim);
      int need = j * R + R; if (need > NK - 1) need = NK - 1;
      wait_stages(issued - 1 - need);
      bar();
      if constexpr (LN == 2) { for (int st = j * R + 1; st <= need; ++st) stats(st); }
    }
    if constexpr (LN == 2) {
      float* st = (float*)(lds + RING);
#pragma unroll
      for (int i = 0; i < XI; ++i) {
        float a = (s1[i][0] + s1[i][1]) + (s1[i][2] + s1[i][3]), b = (s2[i][0] + s2[i][1]) + (s2[i][2] + s2[i][3]);
        a = sum_x4(sum_x2(sum_x1(a))); b = sum_x4(sum_x2(sum_x1(b)));
        float mean = a * (1.f / DM), var = fmaxf(b * (1.f / DM) - mean * mean, 0.f);
        int ci = i * 64 + lane;
        if ((lane & 7) == 0 && ci < XR * 2) { int r = wid * (XR / 4) + (ci >> 3); st[r * 2] = mean; st[r * 2 + 1] = rsqrtf(var + 1e-5f); }
      }
    }
    if constexpr (LN != 0) bar();
  }
  template <class FR> static DEV void stats_from(char* lds, int tid, const float* part, FR xrow) {
    float* st = (float*)(lds + RING);
#pragma unroll
    for (int r = tid >> 2; r < XR; r += 64) {
      const f32x4* p = (const f32x4*)(part + (size_t)xrow(r) * 32 + (tid & 3) * 8);
      f32x4 a = p[0], b = p[1];
      float s = (a[0] + a[2]) + (b[0] + b[2]), q = (a[1] + a[3]) + (b[1] + b[3]);
      s = sum_x2(sum_x1(s)); q = sum_x2(sum_x1(q));
      float mean = s * (1.f / DM), var = fmaxf(q * (1.f / DM) - mean * mean, 0.f);
      if ((tid & 3) == 0) { st[r * 2] = mean; st[r * 2 + 1] = rsqrtf(var + 1e-5f); }
    }
  }
  struct Frags { bf16x8 x[2][MBW], w[2][NBW]; };
  static DEV void run(char* lds, int wid, int lane, int xr0, const int (&wu)[NBW], f32x4 (&acc)[NBW][MBW], f32x4 (&cs)[NBW], int kid) {
    const int g = lane >> 4, q = (lane & 15) >> 2, p = lane & 3;
    lds_cptr L = (lds_cptr)lds;
    const int xa0 = (xr0 + (lane & 15)) * 128 + ((g ^ (lane & 7)) << 4);
    int wa[NBW];
#pragma unroll
    for (int nb = 0; nb < NBW; ++nb) wa[nb] = XB + (8 * g + q) * WRB + ((wu[nb] ^ wswz<WRB>(8 * g + q)) << 5) + p * 8;
    bf16x8 ones;
#pragma unroll
    for (int j = 0; j < 8; ++j) ones[j] = 0x3F80;
    int sb = 0;
    auto ld = [&](Frags& f) {
#pragma unroll
      for (int ks = 0; ks < 2; ++ks) {
#pragma unroll
        for (int nb = 0; nb < NBW; ++nb) {
          s16x4 lo = trread(L + sb + wa[nb] + (32 * ks) * WRB), hi = trread(L + sb + wa[nb] + (32 * ks + 4) * WRB);
          f.w[ks][nb] = __builtin_shufflevector(lo, hi, 0, 1, 2, 3, 4, 5, 6, 7);
        }
#pragma unroll
        for (int mb = 0; mb < MBW; ++mb)
          f.x[ks][mb] = *(const __attribute__((address_space(3))) bf16x8*)(L + sb + ((xa0 ^ (ks * 64)) + mb * 2048));
      }
      sb += STG; if (sb == RING) sb = 0;
    };
    auto mm = [&](const Frags& f) {
#pragma unroll
      for (int ks = 0; ks < 2; ++ks)
#pragma unroll
        for (int nb = 0; nb < NBW; ++nb) {
#pragma unroll
          for (int mb = 0; mb < MBW; ++mb) {
            if (VT && nb == NBW - 1) acc[nb][mb] = MFMA16(f.x[ks][mb], f.w[ks][nb], acc[nb][mb]);
            else acc[nb][mb] = MFMA16(f.w[ks][nb], f.x[ks][mb], acc[nb][mb]);
          }
          if constexpr (LN != 0) {
            if (VT && nb == NBW - 1) cs[nb] = MFMA16(ones, f.w[ks][nb], cs[nb]);
            else cs[nb] = MFMA16(f.w[ks][nb], ones, cs[nb]);
          }
        }
    };
    constexpr int NM = 2 * NBW * (MBW + (LN != 0 ? 1 : 0)), NRD = 2 * (2 * NBW + MBW), RPM = (NRD + NM - 1) / NM;
    auto step = [&](Frags& nx, const Frags& cu) {
      SB0(); ld(nx); mm(cu);
#pragma unroll
      for (int i = 0; i < NM; ++i) { __builtin_amdgcn_sched_group_barrier(0x008, 1, 0); __builtin_amdgcn_sched_group_barrier(0x100, RPM, 0); }
      SB0();
    };
    Frags A, B;
    bar(); ld(A);
#pragma unroll 1
    for (int s = 0; s + 2 < NK; s += 2) {
      bar(); step(B, A);
      if constexpr (R == 1) bar();
      step(A, B);
    }
    if constexpr (NK % 2 == 0) { bar(); step(B, A); if constexpr (R == 1) bar(); mm(B); }
    else { bar(); mm(A); }
    SB0();
    if constexpr (LN != 0) bar();
  }
};

DEV void tile_of(int& rt, int& ct) { int b = blockIdx.x, xcd = b & 7, idx = b >> 3; rt = xcd * 4 + (idx & 3); ct = idx >> 2; }
DEV void warm_share(const void* p, int bytes, int w4, int lane, unsigned junk) { const int sh = bytes / 128; l2_touch((const char*)p + (size_t)((blockIdx.x >> 3) * 4 + w4) * sh, sh, lane, junk); }

DEV s16x4 pack4(f32x4 v) { u32x2 p = {cvtpk(v[0], v[1]), cvtpk(v[2], v[3])}; return __builtin_bit_cast(s16x4, p); }
#define MFMA16K16(a, b, c) __builtin_amdgcn_mfma_f32_16x16x16bf16_1k(a, b, c, 0, 0, 0)
template <int LNM>
DEV void k1_body(const u16* __restrict__ x, const float* __restrict__ part, const u16* __restrict__ wqkv, u16* __restrict__ o, u64* zero_buf, const u16* __restrict__ wnext, const u16* w2, const u16* w3, const u16* w4, int kid) {
  if (threadIdx.x == 0) STAMP(kid, 0);
  using C = Core<80, 384, 8, 4, 1, 5, 3, LNM, 0, true>;
  __shared__ __attribute__((aligned(16))) char lds[C::LDS_BYTES];
  const int tid = threadIdx.x, lane = tid & 63, wid = __builtin_amdgcn_readfirstlane(tid >> 6);
  int rt, ct; tile_of(rt, ct); const int r0 = rt * 64;
  C::flags_init(lds);
  if (zero_buf && tid < 128) zero_buf[blockIdx.x * 128 + tid] = 0ull;
  if (wid >= 4) {
    C c; c.init((unsigned)(uintptr_t)lds, wid - 4, lane, x, DM, wqkv, 3 * DM,
              [&](int r) { int gr = r0 - 16 + r; return gr < 0 ? 0 : gr; },
              [&](int u) { return (u >> 2) * DM + (ct * 4 + (u & 3)) * 16; });
    c.load_loop(lds, wid - 4, kid);
    if constexpr (LNM == 2) {
      const unsigned junk = (unsigned)(uintptr_t)lds + C::JUNK; const int part_ = blockIdx.x * 4 + wid - 4;
      touch_always((const char*)wnext + (size_t)part_ * 512, 512, lane, junk);
      if (part_ < 688) touch_always((const char*)w2 + (size_t)part_ * 2048, 2048, lane, junk);
      touch_always((const char*)w3 + (size_t)part_ * 8192, 8192, lane, junk);
      if (part_ < 688) touch_always((const char*)w4 + (size_t)part_ * 1024, 1024, lane, junk);
    }
    if (wid == 4) STAMP(kid, 2);
    return;
  }
  if constexpr (LNM == 1) C::stats_from(lds, tid, part, [&](int r) { int gr = r0 - 16 + r; return gr < 0 ? 0 : gr; });
  if constexpr (LNM == 2) {
    const unsigned junk = (unsigned)(uintptr_t)lds + C::JUNK;
    const int off = (((blockIdx.x >> 3) * 4 + wid) * 64 + lane) * 128;
    auto warm = [&](unsigned lo, unsigned hi, int bytes) {
      if (off < bytes) {
        const char* p = (const char*)(((u64)hi << 32) | lo) + off; unsigned sv;
        asm volatile("s_mov_b32 %0, m0\n\ts_mov_b32 m0, %2\n\ts_nop 0\n\tglobal_load_lds_dword %1, off\n\ts_mov_b32 m0, %0" : "=&s"(sv) : "v"(p), "s"(junk) : "memory");
      }
    };
#define CODE_ADDR(SYM, BYTES) do { unsigned lo_, hi_; asm volatile("s_getpc_b64 s[40:41]\n\ts_add_u32 s40, s40, " #SYM "@rel32@lo+4\n\ts_addc_u32 s41, s41, " #SYM "@rel32@hi+12\n\ts_mov_b32 %0, s40\n\ts_mov_b32 %1, s41" : "=s"(lo_), "=s"(hi_) :: "s40", "s41", "scc"); warm(lo_, hi_, BYTES); } while (0)
    CODE_ADDR(k2_wo, CW_K2); CODE_ADDR(k3_win, CW_K3); CODE_ADDR(k4_wsp, CW_K4); CODE_ADDR(k5_wout, CW_K5); CODE_ADDR(k1_next, CW_K1);
  }
  f32x4 acc[3][5] = {}, cs[3] = {};
  const int wu[3] = {wid, 4 + wid, 8 + wid};
  C::run(lds, wid, lane, 0, wu, acc, cs, kid); if (wid == 0) STAMP(kid, 3);
  const float* stt = (const float*)(lds + C::RING);
  const int g = lane >> 4, m16 = lane & 15;
  s16x4 qf[5], kf[5], vf[5];
#pragma unroll
  for (int mb = 0; mb < 5; ++mb) {
    const float rs = stt[(mb * 16 + m16) * 2 + 1], cm = -rs * stt[(mb * 16 + m16) * 2];
    if (mb > 0) qf[mb] = pack4(rs * acc[0][mb] + cm * cs[0]);
    kf[mb] = pack4(rs * acc[1][mb] + cm * cs[1]);
    const f32x4 sa = *(const f32x4*)(stt + (mb * 16 + 4 * g) * 2), sb2 = *(const f32x4*)(stt + (mb * 16 + 4 * g) * 2 + 4);
    f32x4 v = {sa[1] * (acc[2][mb][0] - sa[0] * cs[2][0]), sa[3] * (acc[2][mb][1] - sa[2] * cs[2][0]),
               sb2[1] * (acc[2][mb][2] - sb2[0] * cs[2][0]), sb2[3] * (acc[2][mb][3] - sb2[2] * cs[2][0])};
    vf[mb] = pack4(v);
  }
  bool v0[4], v1[4];
#pragma unroll
  for (int j = 0; j < 4; ++j) { int d0 = m16 + 16 - (4 * g + j), d1 = m16 - (4 * g + j); v0[j] = d0 <= 14 && !(d0 & 1); v1[j] = d1 >= 0 && d1 <= 14 && !(d1 & 1); }
  const float sc = 0.25f * 1.4426950408889634f;
#pragma unroll
  for (int qb = 0; qb < 4; ++qb) {
    SB0();
    f32x4 s0 = MFMA16K16(kf[qb], qf[qb + 1], (f32x4{})), s1 = MFMA16K16(kf[qb + 1], qf[qb + 1], (f32x4{}));
    const bool prev_ok = (r0 + qb * 16) > 0;
    float mx = -3.0e38f;
#pragma unroll
    for (int j = 0; j < 4; ++j) {
      s0[j] = (v0[j] && prev_ok) ? s0[j] * sc : -1.0e9f; s1[j] = v1[j] ? s1[j] * sc : -1.0e9f;
      mx = fmaxf(mx, fmaxf(s0[j], s1[j]));
    }
    mx = max_g(mx);
    float sum = 0.f;
#pragma unroll
    for (int j = 0; j < 4; ++j) { s0[j] = __builtin_amdgcn_exp2f(s0[j] - mx); s1[j] = __builtin_amdgcn_exp2f(s1[j] - mx); sum += s0[j] + s1[j]; }
    sum = sum_g(sum);
    const float inv = __builtin_amdgcn_rcpf(sum);
    f32x4 ot = MFMA16K16(vf[qb], pack4(s0 * inv), (f32x4{}));
    ot = MFMA16K16(vf[qb + 1], pack4(s1 * inv), ot);
    *(u32x2*)(o + (size_t)(r0 + qb * 16 + m16) * DM + (ct * 4 + wid) * 16 + 4 * g) = u32x2{cvtpk(ot[0], ot[1]), cvtpk(ot[2], ot[3])};
  }
  if (wid == 0) STAMP(kid, 4);
}

template <int NK, int LDA, int KCLAMP>
DEV void proj_body(const u16* __restrict__ a, const u16* __restrict__ w, const u16* __restrict__ res, u16* __restrict__ out, float* __restrict__ part, const u16* __restrict__ wnext, int wnext_bytes, int kid) {
  if (threadIdx.x == 0) STAMP(kid, 0);
  using C = Core<64, 128, NK, 8, 2, 2, 2, 0, KCLAMP>;
  __shared__ __attribute__((aligned(16))) char lds[C::LDS_BYTES];
  const int tid = threadIdx.x, lane = tid & 63, wid = __builtin_amdgcn_readfirstlane(tid >> 6);
  int rt, ct; tile_of(rt, ct); const int r0 = rt * 64, c0 = ct * 64;
  C::flags_init(lds);
  if (wid >= 4) {
    C c; c.init((unsigned)(uintptr_t)lds, wid - 4, lane, a, LDA, w, DM, [&](int r) { return r0 + r; }, [&](int u) { return c0 + u * 16; });
    c.load_loop(lds, wid - 4, kid);
    if (wnext) warm_share(wnext, wnext_bytes, wid - 4, lane, (unsigned)(uintptr_t)lds + C::JUNK); if (wid == 4) STAMP(kid, 2); return;
  }
  const int wm = wid >> 1, wn = wid & 1, g = lane >> 4, m16 = lane & 15;
  u32x2 rv[2][2];
#pragma unroll
  for (int nb = 0; nb < 2; ++nb)
#pragma unroll
    for (int mb = 0; mb < 2; ++mb) rv[nb][mb] = *(const u32x2*)(res + (size_t)(r0 + wm * 32 + mb * 16 + m16) * DM + c0 + (wn * 2 + nb) * 16 + g * 4);
  f32x4 acc[2][2] = {}, cs[2];
  const int wu[2] = {wn * 2, wn * 2 + 1};
  C::run(lds, wid, lane, wm * 32, wu, acc, cs, kid); if (wid == 0) STAMP(kid, 3);
#pragma unroll
  for (int mb = 0; mb < 2; ++mb) {
    float ps = 0.f, pq = 0.f;
#pragma unroll
    for (int nb = 0; nb < 2; ++nb) {
      f32x4 A = acc[nb][mb]; u32x2 r = rv[nb][mb];
      float v0 = bf2f(r[0] & 0xFFFF) + rbf(A[0]), v1 = bf2f(r[0] >> 16) + rbf(A[1]), v2 = bf2f(r[1] & 0xFFFF) + rbf(A[2]), v3 = bf2f(r[1] >> 16) + rbf(A[3]);
      u32x2 o = {cvtpk(v0, v1), cvtpk(v2, v3)};
      *(u32x2*)(out + (size_t)(r0 + wm * 32 + mb * 16 + m16) * DM + c0 + (wn * 2 + nb) * 16 + g * 4) = o;
      v0 = bf2f(o[0] & 0xFFFF); v1 = bf2f(o[0] >> 16); v2 = bf2f(o[1] & 0xFFFF); v3 = bf2f(o[1] >> 16);
      ps += (v0 + v1) + (v2 + v3); pq += (v0 * v0 + v1 * v1) + (v2 * v2 + v3 * v3);
    }
    ps = sum_g(ps); pq = sum_g(pq);
    if (g == 0) *(float2*)(part + (size_t)(r0 + wm * 32 + mb * 16 + m16) * 32 + (ct * 2 + wn) * 2) = make_float2(ps, pq);
  }
  if (wid == 0) STAMP(kid, 4);
}

typedef __attribute__((ext_vector_type(2))) float f32x2;
DEV f32x2 gelu2(f32x2 t) {
  const float c1 = -1.5957691216057308f * 1.4426950408889634f, c2 = c1 * 0.044715f;
  f32x2 y = t * (t * t * c2 + c1);
  f32x2 d = {__builtin_amdgcn_exp2f(y[0]), __builtin_amdgcn_exp2f(y[1])};
  d += 1.f;
  f32x2 r = {__builtin_amdgcn_rcpf(d[0]), __builtin_amdgcn_rcpf(d[1])};
  return t * r;
}
extern "C" __global__ __launch_bounds__(512, 2) void k3_win(const u16* __restrict__ x, const float* __restrict__ part, const u16* __restrict__ w_in, u16* __restrict__ z1, u16* __restrict__ z2n, u64* xch, unsigned tag, const u16* __restrict__ w_sp, int kid) {
  if (threadIdx.x == 0) STAMP(kid, 0);
  using C = Core<64, 384, 8, 4, 1, 4, 3, 1, 0>;
  __shared__ __attribute__((aligned(16))) char lds[C::LDS_BYTES];
  const int tid = threadIdx.x, lane = tid & 63, wid = __builtin_amdgcn_readfirstlane(tid >> 6), w4 = wid & 3;
  int rt, ct; tile_of(rt, ct); const int r0 = rt * 64;
  C::flags_init(lds);
  const int half = ct >> 2, zt = ct & 3, nun = zt == 3 ? 10 : 11, f0 = zt * 176;
  f32x4 a2[3][2], cs[3];
  f32x4* ho = (f32x4*)(lds + 4096) + w4 * (9 * 64) + lane;
  if (wid >= 4) {
    C c; c.init((unsigned)(uintptr_t)lds, wid - 4, lane, x, DM, w_in, FF, [&](int r) { return r0 + r; },
              [&](int u) { return half * FH + f0 + (u < nun ? u : 0) * 16; });
    c.load_loop(lds, wid - 4, kid);
    if (wid == 4) STAMP(kid, 2);
    bar();
#pragma unroll
    for (int nb = 0; nb < 3; ++nb) { a2[nb][0] = ho[(nb * 3) * 64]; a2[nb][1] = ho[(nb * 3 + 1) * 64]; cs[nb] = ho[(nb * 3 + 2) * 64]; }
  } else {
    C::stats_from(lds, tid, part, [&](int r) { return r0 + r; });
    f32x4 acc[3][4] = {};
#pragma unroll
    for (int nb = 0; nb < 3; ++nb) cs[nb] = f32x4{};
    const int wu[3] = {wid * 3, wid * 3 + 1, wid * 3 + 2};
    C::run(lds, wid, lane, 0, wu, acc, cs, kid); if (wid == 0) STAMP(kid, 3);
#pragma unroll
    for (int nb = 0; nb < 3; ++nb) { ho[(nb * 3) * 64] = acc[nb][2]; ho[(nb * 3 + 1) * 64] = acc[nb][3]; ho[(nb * 3 + 2) * 64] = cs[nb]; a2[nb][0] = acc[nb][0]; a2[nb][1] = acc[nb][1]; }
    bar();
  }
  const float* stt = (const float*)(lds + C::RING);
  const int g = lane >> 4, m16 = lane & 15, rb = (wid >> 2) * 32;
  u32x2 pk[3][2]; float p1[2], p2[2];
#pragma unroll
  for (int mb = 0; mb < 2; ++mb) {
    const float rs = stt[(rb + mb * 16 + m16) * 2 + 1], cm = -rs * stt[(rb + mb * 16 + m16) * 2];
    f32x2 s1 = {0.f, 0.f}, s2 = {0.f, 0.f};
#pragma unroll
    for (int nb = 0; nb < 3; ++nb) {
      f32x4 t = rs * a2[nb][mb] + cm * cs[nb];
      f32x2 lo = gelu2(f32x2{t[0], t[1]}), hi = gelu2(f32x2{t[2], t[3]});
      pk[nb][mb] = u32x2{cvtpk(lo[0], lo[1]), cvtpk(hi[0], hi[1])};
      if (w4 * 3 + nb < nun) {
        f32x2 a = {bf2f(pk[nb][mb][0] & 0xFFFF), bf2f(pk[nb][mb][0] >> 16)}, b = {bf2f(pk[nb][mb][1] & 0xFFFF), bf2f(pk[nb][mb][1] >> 16)};
        s1 += a + b; s2 += a * a + b * b;
      }
    }
    p1[mb] = s1[0] + s1[1]; p2[mb] = s2[0] + s2[1];
  }
  if (half == 0) {
#pragma unroll
    for (int mb = 0; mb < 2; ++mb)
#pragma unroll
      for (int nb = 0; nb < 3; ++nb)
        if (w4 * 3 + nb < nun) *(u32x2*)(z1 + (size_t)(r0 + rb + mb * 16 + m16) * FH + f0 + (w4 * 3 + nb) * 16 + g * 4) = pk[nb][mb];
    if (wid == 0) STAMP(kid, 4);
    return;
  }
  if (wid == 0) STAMP(kid, 5);
  float* red = (float*)lds;
  float* tot = red + 512;
#pragma unroll
  for (int mb = 0; mb < 2; ++mb) {
    float a = p1[mb], b = p2[mb];
    a = sum_g(a); b = sum_g(b);
    if (g == 0) *(float2*)(red + (w4 * 64 + rb + mb * 16 + m16) * 2) = make_float2(a, b);
  }
  __syncthreads();
  if (tid < 128) {
    float own = (red[tid] + red[128 + tid]) + (red[256 + tid] + red[384 + tid]);
    u64* base = xch + (size_t)rt * 4 * 128;
    __hip_atomic_store(base + zt * 128 + tid, ((u64)tag << 32) | __float_as_uint(own), __ATOMIC_RELAXED, __HIP_MEMORY_SCOPE_AGENT);
    u64 v[4]; int spins = 0; bool ok;
    do {
#pragma unroll
      for (int t = 0; t < 4; ++t) v[t] = __hip_atomic_load(base + t * 128 + tid, __ATOMIC_RELAXED, __HIP_MEMORY_SCOPE_AGENT);
      ok = true;
#pragma unroll
      for (int t = 0; t < 4; ++t) ok = ok && (t == zt || (unsigned)(v[t] >> 32) == tag);
    } while (!ok && ++spins < (1 << 22));
    float pt[4];
#pragma unroll
    for (int t = 0; t < 4; ++t) pt[t] = t == zt ? own : __uint_as_float((unsigned)v[t]);
    tot[tid] = (pt[0] + pt[1]) + (pt[2] + pt[3]);
  }
  __syncthreads();
  if (wid == 0) STAMP(kid, 6);
#pragma unroll
  for (int mb = 0; mb < 2; ++mb) {
    const int r = rb + mb * 16 + m16;
    const float mean = tot[r * 2] * (1.f / FH), var = fmaxf(tot[r * 2 + 1] * (1.f / FH) - mean * mean, 0.f);
    const float rs = rsqrtf(var + 1e-5f), cm = -mean * rs;
#pragma unroll
    for (int nb = 0; nb < 3; ++nb) {
      const int f = f0 + (w4 * 3 + nb) * 16;
      if (w4 * 3 + nb < nun) {
        u32x2 v = pk[nb][mb];
        *(u32x2*)(z2n + ((size_t)(f / 96) * SEQ + r0 + r) * 96 + f % 96 + g * 4) =
            u32x2{cvtpk(fmaf(bf2f(v[0] & 0xFFFF), rs, cm), fmaf(bf2f(v[0] >> 16), rs, cm)), cvtpk(fmaf(bf2f(v[1] & 0xFFFF), rs, cm), fmaf(bf2f(v[1] >> 16), rs, cm))};
      }
    }
  }
  if (wid == 0) STAMP(kid, 4);
}

extern "C" __global__ __launch_bounds__(512, 2) void k4_wsp(const u16* __restrict__ w_sp, const u16* __restrict__ z2n, const u16* __restrict__ z1, const u16* __restrict__ b_sp, u16* __restrict__ u, const u16* __restrict__ wnext, int kid) {
  if (threadIdx.x == 0) STAMP(kid, 0);
  using C = Core<64, 192, 32, 7, 2, 2, 3, 0, 0>;
  __shared__ __attribute__((aligned(16))) char lds[C::LDS_BYTES];
  const int tid = threadIdx.x, lane = tid & 63, wid = __builtin_amdgcn_readfirstlane(tid >> 6);
  int rt, ct; tile_of(rt, ct); const int r0 = rt * 64, c0 = ct * 96;
  C::flags_init(lds);
  if (wid >= 4) {
    C c; c.init((unsigned)(uintptr_t)lds, wid - 4, lane, w_sp, SEQ, z2n + (size_t)ct * SEQ * 96, 96, [&](int r) { return r0 + r; },
              [&](int uu) { return uu * 16; });
    c.load_loop(lds, wid - 4, kid);
    warm_share(wnext, FH * DM * 2, wid - 4, lane, (unsigned)(uintptr_t)lds + C::JUNK); if (wid == 4) STAMP(kid, 2); return;
  }
  const int wm = wid >> 1, wn = wid & 1, g = lane >> 4, m16 = lane & 15;
  u32x2 zv[3][2]; float bv[2];
#pragma unroll
  for (int mb = 0; mb < 2; ++mb) {
    bv[mb] = bf2f(b_sp[r0 + wm * 32 + mb * 16 + m16]);
#pragma unroll
    for (int nb = 0; nb < 3; ++nb) {
      int col = c0 + (wn * 3 + nb) * 16; if (col >= FH) col = 0;
      zv[nb][mb] = *(const u32x2*)(z1 + (size_t)(r0 + wm * 32 + mb * 16 + m16) * FH + col + g * 4);
    }
  }
  f32x4 acc[3][2] = {}, cs[3];
  const int wu[3] = {wn * 3, wn * 3 + 1, wn * 3 + 2};
  C::run(lds, wid, lane, wm * 32, wu, acc, cs, kid); if (wid == 0) STAMP(kid, 3);
#pragma unroll
  for (int nb = 0; nb < 3; ++nb) {
    const int col = c0 + (wn * 3 + nb) * 16;
#pragma unroll
    for (int mb = 0; mb < 2; ++mb) {
      f32x4 A = acc[nb][mb]; u32x2 z = zv[nb][mb]; float b = bv[mb];
      float v0 = bf2f(z[0] & 0xFFFF) * rbf(rbf(A[0]) + b), v1 = bf2f(z[0] >> 16) * rbf(rbf(A[1]) + b);
      float v2 = bf2f(z[1] & 0xFFFF) * rbf(rbf(A[2]) + b), v3 = bf2f(z[1] >> 16) * rbf(rbf(A[3]) + b);
      u32x2 r = {cvtpk(v0, v1), cvtpk(v2, v3)};
      if (col >= FH) r = u32x2{0u, 0u};
      if (col < UP) *(u32x2*)(u + (size_t)(r0 + wm * 32 + mb * 16 + m16) * UP + col + g * 4) = r;
    }
  }
  if (wid == 0) STAMP(kid, 4);
}

#define K1_ARGS const u16* x, const float* part, const u16* wqkv, u16* o, u64* zero_buf, const u16* wnext, const u16* w2, const u16* w3, const u16* w4, int kid
extern "C" __global__ __launch_bounds__(512, 2) void k1_first(K1_ARGS) { k1_body<2>(x, part, wqkv, o, zero_buf, wnext, w2, w3, w4, kid); }
extern "C" __global__ __launch_bounds__(512, 2) void k1_next(K1_ARGS) { k1_body<1>(x, part, wqkv, o, zero_buf, wnext, w2, w3, w4, kid); }
#define PJ_ARGS const u16* a, const u16* w, const u16* res, u16* out, float* part, const u16* wnext, int wnext_bytes, int kid
extern "C" __global__ __launch_bounds__(512, 2) void k2_wo(PJ_ARGS) { proj_body<8, DM, 0>(a, w, res, out, part, wnext, wnext_bytes, kid); }
extern "C" __global__ __launch_bounds__(512, 2) void k5_wout(PJ_ARGS) { proj_body<11, UP, FH>(a, w, res, out, part, wnext, wnext_bytes, kid); }

extern "C" void kernel_launch(void* const* d_in, const int* in_sizes, int n_in, void* d_out, int out_size, void* d_ws, size_t ws_size, hipStream_t stream) {
  const u16* x = (const u16*)d_in[0]; const u16* wqkv = (const u16*)d_in[1]; const u16* wo = (const u16*)d_in[2];
  const u16* w_in = (const u16*)d_in[4]; const u16* w_sp = (const u16*)d_in[5]; const u16* b_sp = (const u16*)d_in[6]; const u16* w_out = (const u16*)d_in[7];
  char* ws = (char*)d_ws;
  u64* xch = (u64*)ws;
  u16* o = (u16*)(ws + (1 << 20)); u16* x1 = o + SEQ * DM; u16* x2 = x1 + SEQ * DM;
  u16* z1 = x2 + SEQ * DM; u16* z2n = z1 + SEQ * FH; u16* u = z2n + SEQ * 768;
  float* part = (float*)(ws + (512 << 10));
  const u16* xin = x;
  for (int L = 0; L < NLAYERS; ++L) {
    u16* xo = L == 0 ? x2 : (u16*)d_out;
    if (L == 0) k1_first<<<256, 512, 0, stream>>>(xin, part, wqkv, o, xch, wo, w_in, w_sp, w_out, L * 5 + 0);
    else k1_next<<<256, 512, 0, stream>>>(xin, part, wqkv, o, nullptr, wo, w_in, w_sp, w_out, L * 5 + 0);
    k2_wo<<<256, 512, 0, stream>>>(o, wo, xin, x1, part, w_in, DM * FF * 2, L * 5 + 1);
    k3_win<<<256, 512, 0, stream>>>(x1, part, w_in, z1, z2n, xch + (size_t)L * 32 * 4 * 128, 0x5A5A0001u + L, w_sp, L * 5 + 2);
    k4_wsp<<<256, 512, 0, stream>>>(w_sp, z2n, z1, b_sp, u, w_out, L * 5 + 3);
    k5_wout<<<256, 512, 0, stream>>>(u, w_out, x1, xo, part, L == 0 ? wqkv : nullptr, DM * 3 * DM * 2, L * 5 + 4);
    xin = xo;
  }
}
```
